```python
import torch

def _softmax(x, axis=-1):
    return torch.softmax(x, dim=axis)

def _gelu(x):
    return torch.nn.functional.gelu(x, approximate='tanh')

def _silu(x):
    return torch.nn.functional.silu(x)

def _sigmoid(x):
    return torch.sigmoid(x)

def _layernorm(x, axis=-1, eps=1e-05):
    m = x.mean(dim=axis, keepdim=True)
    v = x.var(dim=axis, keepdim=True, unbiased=False)
    return (x - m) / torch.sqrt(v + eps)

def _scatter_add(tgt, idx, src, axis=0):
    return tgt.clone().index_add_(axis, idx, src)

def _segment_sum(src, seg, n):
    out = torch.zeros((n,) + src.shape[1:], dtype=src.dtype)
    return out.index_add_(0, seg, src)

def _pad(x, pw):
    flat = tuple((v for pair in reversed(pw) for v in pair))
    return torch.nn.functional.pad(x, flat)
kernel_name = 'parallel_sum__grouped_kv_mqa__mixer_gmlp__prompt'

def setup_inputs():

    _w = lambda a: (lambda t: t.to(torch.bfloat16) if t.is_floating_point() else t)(torch.as_tensor(a))

    def _A_setup():
        B, S, D, H = (1, 2048, 576, 32)
        Dh = D // H
        return {'x': _w((lambda *s: torch.randn(*s, dtype=torch.bfloat16) * 0.05)(B, S, D)), 'wq': _w((lambda *s: torch.randn(*s, dtype=torch.bfloat16) * 0.2)(D, H * Dh)), 'wk': _w((lambda *s: torch.randn(*s, dtype=torch.bfloat16) * 0.2)(D, Dh)), 'wv': _w((lambda *s: torch.randn(*s, dtype=torch.bfloat16) * 0.1)(D, Dh)), 'wo': _w((lambda *s: torch.randn(*s, dtype=torch.bfloat16) * 0.1)(H * Dh, D))}
    _A_in = _A_setup()

    def _B_setup():
        B, S, D, F = (1, 2048, 576, 4608)
        Fh = F // 2
        return {'x': _w((lambda *s: torch.randn(*s, dtype=torch.bfloat16) * 0.05)(B, S, D)), 'w_in': _w((lambda *s: torch.randn(*s, dtype=torch.bfloat16) * 0.4)(D, 2 * Fh)), 'w_sp': _w(0.01 * (lambda *s: torch.randn(*s, dtype=torch.bfloat16) * 0.4)(S, S)), 'b_sp': _w(1.0 + 0.01 * (lambda *s: torch.randn(*s, dtype=torch.bfloat16) * 0.1)(S)), 'w_out': _w((lambda *s: torch.randn(*s, dtype=torch.bfloat16) * 0.1)(Fh, D))}
    _B_in = _B_setup()
    return {'A_x': _A_in['x'], 'A_wq': _A_in['wq'], 'A_wk': _A_in['wk'], 'A_wv': _A_in['wv'], 'A_wo': _A_in['wo'], 'B_x': _B_in['x'], 'B_w_in': _B_in['w_in'], 'B_w_sp': _B_in['w_sp'], 'B_b_sp': _B_in['b_sp'], 'B_w_out': _B_in['w_out']}

def reference(A_x, A_wq, A_wk, A_wv, A_wo, B_x, B_w_in, B_w_sp, B_b_sp, B_w_out):

    def _A_ref(x, wq, wk, wv, wo):
        B, S, D = x.shape
        H = 32
        Dh = D // H
        q = (lambda x, *d: x.permute(*d))((x @ wq).reshape(B, S, H, Dh), 0, 2, 1, 3)
        k = x @ wk
        v = x @ wv
        scores = torch.einsum('bhqd,bkd->bhqk', q, k) * Dh ** (-0.5)
        p = _softmax(scores, axis=-1)
        o = torch.einsum('bhqk,bkd->bhqd', p, v)
        return (lambda x, *d: x.permute(*d))(o, 0, 2, 1, 3).reshape(B, S, H * Dh) @ wo

    def _B_ref(x, w_in, w_sp, b_sp, w_out):
        B, S, D = x.shape
        F = w_in.shape[-1]
        Fh = F // 2
        z = _gelu(torch.einsum('bsd,df->bsf', x, w_in))
        z1 = z[..., :Fh].clone()
        z2 = _layernorm(z[..., Fh:], axis=-1)
        g = torch.einsum('ts,bsf->btf', w_sp, z2) + b_sp[None, :, None]
        return torch.einsum('bsf,fd->bsd', z1 * g, w_out)
    x = A_x
    xn = _layernorm(x, axis=-1)
    return x + _A_ref(xn, A_wq, A_wk, A_wv, A_wo) + _B_ref(xn, B_w_in, B_w_sp, B_b_sp, B_w_out)
```

```cpp
#include <hip/hip_runtime.h>
#include <hip/hip_bf16.h>
#include <type_traits>

typedef unsigned short u16;
using bf16x8 = __attribute__((ext_vector_type(8))) short;
using bf16x4 = __attribute__((ext_vector_type(4))) short;
using f32x2 = __attribute__((ext_vector_type(2))) float;
using f32x4 = __attribute__((ext_vector_type(4))) float;
using f32x16 = __attribute__((ext_vector_type(16))) float;
using u32x4 = __attribute__((ext_vector_type(4))) unsigned;
using u32x2 = __attribute__((ext_vector_type(2))) unsigned;
typedef __bf16 bf16x2_t __attribute__((ext_vector_type(2)));
typedef short v4i16_t __attribute__((ext_vector_type(4)));
typedef __attribute__((address_space(1))) unsigned gu32;
typedef __attribute__((address_space(1))) unsigned long long gu64;
#define LAS __attribute__((address_space(3)))
#define DI __device__ __forceinline__
#define SBAR() __builtin_amdgcn_sched_barrier(0)
#define MF32(a,b,c) __builtin_amdgcn_mfma_f32_32x32x16_bf16(__builtin_bit_cast(bf16x8,a),__builtin_bit_cast(bf16x8,b),c,0,0,0)
#define MF16(a,b,c) __builtin_amdgcn_mfma_f32_16x16x32_bf16(__builtin_bit_cast(bf16x8,a),__builtin_bit_cast(bf16x8,b),c,0,0,0)
#define MF32K8(a,b,c) __builtin_amdgcn_mfma_f32_32x32x8bf16_1k(__builtin_bit_cast(bf16x4,a),__builtin_bit_cast(bf16x4,b),c,0,0,0)
#define MF16A(c,a,b) asm volatile("v_mfma_f32_16x16x32_bf16 %0, %1, %2, %0" : "+v"(c) : "v"(a),"v"(b))
#define MFMA_DRAIN() asm volatile("s_nop 15\n\ts_nop 3":::"memory")
#define LD(p) (*(const LAS u32x4*)(p))
#define WAIT_LGKM0() __builtin_amdgcn_s_waitcnt(0xC07F)
#define WAIT_VM0() asm volatile("s_waitcnt vmcnt(0)":::"memory")
#define PIN(x) asm volatile("":"+v"(x))
#define BARRIER() do{ asm volatile("":::"memory"); __builtin_amdgcn_s_barrier(); asm volatile("":::"memory"); }while(0)
#define RLX_AGENT __ATOMIC_RELAXED, __HIP_MEMORY_SCOPE_AGENT
#define ONES 0x3f803f80u

DI unsigned cvt2(float a,float b){ f32x2 v={a,b}; return __builtin_bit_cast(unsigned,__builtin_convertvector(v,bf16x2_t)); }
DI float bflo(unsigned v){ return __uint_as_float(v<<16); }
DI float bfhi(unsigned v){ return __uint_as_float(v&0xffff0000u); }
DI float dot2(unsigned a,unsigned b,float c){ return __builtin_amdgcn_fdot2_f32_bf16(__builtin_bit_cast(bf16x2_t,a),__builtin_bit_cast(bf16x2_t,b),c,false); }
DI u32x2 trd(const LAS char* p){ return __builtin_bit_cast(u32x2,__builtin_amdgcn_ds_read_tr16_b64_v4i16((LAS v4i16_t*)p)); }
DI int swap23(int k){ return (k&~12)|((k&4)<<1)|((k&8)>>1); }
DI void glds(const char* sbase,unsigned voff,unsigned m0v){
  asm volatile("s_mov_b32 m0, %2\n\ts_nop 0\n\tglobal_load_lds_dwordx4 %1, %0" :: "s"(sbase),"v"(voff),"s"(m0v) : "memory"); }
DI void glds2(const char* sbase,unsigned voff,unsigned m0v){
  asm volatile("s_mov_b32 m0, %2\n\ts_nop 0\n\tglobal_load_lds_dwordx4 %1, %0\n\tglobal_load_lds_dwordx4 %1, %0 offset:1024" :: "s"(sbase),"v"(voff),"s"(m0v) : "memory"); }
template<int I> DI void eglds(int w,const char* sbase,unsigned voff,unsigned m0v){
  asm volatile("s_cmp_lg_u32 %3, %4\n\ts_cbranch_scc1 EG%=\n\ts_mov_b32 m0, %2\n\ts_nop 0\n\tglobal_load_lds_dwordx4 %1, %0\nEG%=:"
               :: "s"(sbase),"v"(voff),"s"(m0v),"s"(w),"i"(I) : "memory","scc"); }
#define WAIT_VM(n) asm volatile("s_waitcnt vmcnt(%0)"::"i"(n):"memory")
DI float ld4(const void* p){ return __uint_as_float(__hip_atomic_load((gu32*)p,RLX_AGENT)); }
DI f32x2 ld8(const void* p){ unsigned long long v=__hip_atomic_load((gu64*)p,RLX_AGENT); return (f32x2){__uint_as_float((unsigned)v),__uint_as_float((unsigned)(v>>32))}; }
template<int I> DI void cglds(int n,const char* sbase,unsigned voff,unsigned m0v){
  asm volatile("s_cmp_le_i32 %3, %4\n\ts_cbranch_scc1 CG%=\n\ts_mov_b32 m0, %2\n\ts_nop 0\n\tglobal_load_lds_dwordx4 %1, %0\nCG%=:"
               :: "s"(sbase),"v"(voff),"s"(m0v),"s"(n),"i"(I) : "memory","scc"); }
DI void wait_vm_sel(int sel){
  asm volatile("s_cmp_eq_u32 %0, 1\n\ts_cbranch_scc1 WA%=\n\ts_cmp_eq_u32 %0, 2\n\ts_cbranch_scc1 WB%=\n\ts_cmp_eq_u32 %0, 3\n\ts_cbranch_scc1 WC%=\n\t"
               "s_waitcnt vmcnt(0)\nWA%=:\n\ts_waitcnt vmcnt(8)\nWB%=:\n\ts_waitcnt vmcnt(10)\nWC%=:" :: "s"(sel) : "memory","scc"); }
DI void st16(void* p,u32x4 v){ asm volatile("global_store_dwordx4 %0, %1, off sc1\n\ts_nop 1" :: "v"(p),"v"(v) : "memory"); }
DI void st8(void* p,u32x2 v){ __hip_atomic_store((gu64*)p,(unsigned long long)v[0]|((unsigned long long)v[1]<<32),RLX_AGENT); }
DI void st4(void* p,unsigned v){ __hip_atomic_store((gu32*)p,v,RLX_AGENT); }

DI u32x4 pair16(u32x2 a,u32x2 b){
  auto s0=__builtin_amdgcn_permlane16_swap(a[0],b[0],false,false); auto s1=__builtin_amdgcn_permlane16_swap(a[1],b[1],false,false);
  return (u32x4){s0[0],s1[0],s0[1],s1[1]}; }

constexpr int S=2048, D=576, FH=2304, FF=4608;
constexpr int TST=16384, FST1=18432;
constexpr int KV_K0=0, KV_K1=4096, KV_V=5120, VT=1216, KVCH=9984, NCH=16;
constexpr float QSCALE=0.23570226039551584f*1.4426950408889634f;
constexpr int LDS_BYTES=81920;

constexpr size_t WS_FLAG=0;
constexpr size_t WS_LN=16384;
constexpr size_t WS_CS=WS_LN+16384;
constexpr size_t WS_WOF=WS_CS+32768;
constexpr size_t WS_Z2F=WS_WOF+(size_t)90*36*1024;
constexpr size_t WS_Q=WS_Z2F+(size_t)16*32*FST1;
constexpr size_t WS_KV=WS_Q+(size_t)S*D*2;
constexpr size_t WS_ST=WS_KV+163840;
constexpr size_t WS_WPF=WS_ST+(size_t)S*32*8;
constexpr size_t WS_CP=WS_WPF+(size_t)16*32*TST;
constexpr size_t WS_HF=WS_CP+(size_t)16*S*4;
constexpr size_t WS_END=WS_HF+(size_t)16*45*TST;
enum { FQ=0, FS=1, FW=2, FHH=3, FO=4, FL=5 };

#define TS(kern,i)

struct In { const u16 *x,*wq,*wk,*wv,*wo,*w_in,*w_sp,*b_sp,*w_out; };

DI f32x2 gelu2(f32x2 x){
  const float A=-2.3022082f, Bc=-0.10294324f;
  f32x2 x2=x*x; f32x2 t=x2*Bc+A; f32x2 w=x*t;
  f32x2 e; e.x=__builtin_amdgcn_exp2f(w.x); e.y=__builtin_amdgcn_exp2f(w.y);
  e=e+1.0f; f32x2 r; r.x=__builtin_amdgcn_rcpf(e.x); r.y=__builtin_amdgcn_rcpf(e.y);
  return x*r; }
#define DPPADD(v,ctrl) v+=__builtin_bit_cast(float,__builtin_amdgcn_update_dpp(0,__builtin_bit_cast(int,v),ctrl,0xf,0xf,false))
DI float row16_sum(float v){ DPPADD(v,0x128); DPPADD(v,0x124); DPPADD(v,0x122); DPPADD(v,0x121); return v; }
DI float gsum(float v){ v+=__shfl_xor(v,16); v+=__shfl_xor(v,32); return v; }
DI void tile_map(int b,int& tb,int& fb){ const int xcd=b&7, idx=b>>3; tb=(xcd&3)*4+(idx&3); fb=(xcd>>2)*8+(idx>>2); }
DI int tile_id(int tb,int fb){ return ((tb>>2)+4*(fb>>3))+8*((tb&3)+4*(fb&7)); }

DI void set_flag(char* ws,int id,int which,unsigned tag){ __hip_atomic_store((gu32*)(ws+WS_FLAG)+id*16+which,tag,RLX_AGENT); }
DI void poll_flags(char* ws,int id,int which,unsigned tag){
  gu32* p=(gu32*)(ws+WS_FLAG)+id*16+which;
  for(unsigned sp=0; sp<(1u<<16); sp++){ unsigned v=__hip_atomic_load(p,RLX_AGENT); if(__all(v==tag)) break; __builtin_amdgcn_s_sleep(4); } }
DI void poll_stats(char* ws,int lane,unsigned tag){
  poll_flags(ws,tile_id(lane&15,lane>>4),FL,tag); poll_flags(ws,lane<18? tile_id(lane&15,4+(lane>>4)) : tile_id(lane&15,14+((lane>>4)&1)),FL,tag); }
#define ACQUIRE() __builtin_amdgcn_fence(__ATOMIC_ACQUIRE,"agent")
DI void wait_flags(char* ws,int id,int which,unsigned tag){ poll_flags(ws,id,which,tag); ACQUIRE(); }

struct Acc { f32x4 m[4][4]; f32x4 s[2]; };
struct Frg { u32x4 t[4], f[4], s; };
DI void acc_zero(Acc& a){
#pragma unroll
  for(int i=0;i<4;i++)
#pragma unroll
    for(int j=0;j<4;j++){ a.m[i][j]=(f32x4){0,0,0,0}; PIN(a.m[i][j]); }
  a.s[0]=(f32x4){0,0,0,0}; a.s[1]=(f32x4){0,0,0,0}; PIN(a.s[0]); PIN(a.s[1]); }
constexpr int G_F=32768, G_SC=G_F+4*9216;

#define CY(v)
#define CYACC()
#define CYDUMP(k)
#define CYSTART()
DI void g_role(const In& in,char* ws,u16* out,char* lds,int id,int wid,int lane,unsigned tag){
  int tb,fb; tile_map(id,tb,fb);
  const int wt=wid&1, wf=wid>>1, r=lane&15, g=lane>>4, lr=lane>>3;
  const LAS char* L=(const LAS char*)lds;
  const unsigned lds0=(unsigned)(size_t)lds, vF=lane*16;
  unsigned vT[2], vW[3]; const char *gT, *gF;
  vT[0]=lr*1152+(((lane&7)^(lr>>1))*16); vT[1]=lr*1152+(((lane&7)^((lr>>1)+4))*16);
#pragma unroll
  for(int i=0;i<3;i++){ const int c=(i<2? wid*2+i : 8)*64+lane, slot=c/18, ch=c-slot*18; vW[i]=swap23(slot)*(FF*2)+ch*16; }
  const unsigned lT=lds0+wid*4096, lTf=lds0+wid*2048, lF=lds0+G_F+wid*2048, lF8=lds0+G_F+8192;
#define DT(q,i) glds(gT+(i)*9216,vT[(i)&1],lT+((q)>>1)*16384+(i)*1024)
#define DW(q,i) glds(gF,vW[i],lF+(q)*9216+(i)*1024)
#define DW8(q) eglds<q>(wid,gF,vW[2],lF8+(q)*9216)
#define FT(q) glds2(gT,vF,lTf+(q)*8192)
#define FW_(q) glds2(gF+wid*2048,vF,lF+(q)*9216)
#define FW8(q) eglds<q>(wid,gF+8192,vF,lF8+(q)*9216)
#define RAW_POS(q) do{ if((q)&1){ DT(q,0); DT(q,1); DT(q,2); DT(q,3); gT+=128; } DW(q,0); DW(q,1); DW8(q); gF+=32*FF*2; }while(0)
#define FO_POS(q) do{ FT(q); FW_(q); FW8(q); gT+=8192; gF+=9216; }while(0)
  auto raw_start=[&](int col0){ gT=(const char*)(in.x+(long)(tb*128+wid*32)*D); gF=(const char*)(in.w_in+col0);
    DT(3,0); DT(3,1); DT(3,2); DT(3,3); gT+=128; DW(2,0); DW(2,1); DW8(2); gF+=32*FF*2; DW(3,0); DW(3,1); DW8(3); gF+=32*FF*2; RAW_POS(0); RAW_POS(1); };
  const LAS char *tr_[2][2], *tf_[2], *frm[2], *ffm[2];
  const int sl=(r>>2)+4*(g&1)+16*(g>>1);
#pragma unroll
  for(int p=0;p<2;p++){ const int u=wt*4+((2*p+2*wf)&3); tf_[p]=L+u*1024+lane*16;
#pragma unroll
    for(int k=0;k<2;k++) tr_[k][p]=L+(u*16+r)*128+(((4*k+g)^((r>>1)&7))*16);
    const int v=wf*4+((2*p+2*wt)&3); frm[p]=L+G_F+sl*288+(lane&3)*8+v*32; ffm[p]=L+G_F+v*1024+lane*16; }
  const LAS char* frs=L+G_F+sl*288+(lane&3)*8+256; const LAS char* ffs=L+G_F+8192+lane*16;
#define FU(fi) (wf*4+(((fi)+2*wt)&3))
  Acc acc; Frg fa,fb_;
  unsigned long long c_a=0,c_b=0,c_c=0,c_d=0,c_s=0,a_l=0,a_v=0,a_b=0;
#define RH(dst,h,ptr) do{ u32x2 v_=trd(ptr); dst[2*(h)]=v_[0]; dst[2*(h)+1]=v_[1]; }while(0)
#define RT(X,q,i) do{ if(MODE<2) X.t[i]=LD(tr_[(q)&1][(i)>>1]+((q)>>1)*16384+((i)&1)*2048); else X.t[i]=LD(tf_[(i)>>1]+(q)*8192+((i)&1)*1024); }while(0)
#define RF(X,q,j) do{ if(MODE<2){ RH(X.f[j],0,frm[(j)>>1]+(q)*9216+((j)&1)*32); RH(X.f[j],1,frm[(j)>>1]+(q)*9216+8*288+((j)&1)*32); } else X.f[j]=LD(ffm[(j)>>1]+(q)*9216+((j)&1)*1024); }while(0)
#define RS(X,q) do{ if(MODE<2){ RH(X.s,0,frs+(q)*9216); RH(X.s,1,frs+(q)*9216+8*288); } else X.s=LD(ffs+(q)*9216); }while(0)
#define RDALL(X,q) do{ _Pragma("unroll") for(int i=0;i<4;i++){ RT(X,q,i); RF(X,q,i); } RS(X,q); }while(0)
#define MM(fi,ti,X) do{ if(MODE==0) MF16A(acc.m[fi][ti],X.t[ti],X.f[fi]); else MF16A(acc.m[fi][ti],X.f[fi],X.t[ti]); }while(0)
#define MS(ti,X) do{ if(MODE==0) MF16A(acc.s[ti],X.t[ti],X.s); else MF16A(acc.s[ti],X.s,X.t[ti]); }while(0)
#define GG(MF,W) do{ MF; SBAR(); W; SBAR(); }while(0)
#define STEP(X,Y,q,DMA,VM) do{ constexpr int qn=((q)+1)&3; constexpr bool OT=(DMA)&&MODE<2&&((q)&1); \
    SBAR(); CY(c_a); WAIT_LGKM0(); CY(c_b); MM(0,0,X); MM(0,1,X); WAIT_VM(VM); CY(c_c); BARRIER(); CY(c_d); CYACC(); SBAR(); \
    GG(MM(0,2,X),RT(Y,qn,0); RF(Y,qn,0)); GG(MM(0,3,X),RT(Y,qn,1); RT(Y,qn,2)); GG(MM(1,0,X),RT(Y,qn,3); RF(Y,qn,1)); GG(MM(1,1,X),RF(Y,qn,2)); \
    GG(MM(1,2,X),RF(Y,qn,3)); GG(MM(1,3,X),RS(Y,qn)); \
    GG(MM(2,0,X),if(OT) DT(q,0); else if(DMA && MODE==2) FT(q)); GG(MM(2,1,X),(void)0); GG(MM(2,2,X),if(OT) DT(q,1)); GG(MM(2,3,X),if(DMA && MODE==2) FW_(q)); \
    GG(MM(3,0,X),if(OT) DT(q,2)); GG(MM(3,1,X),if(OT) DT(q,3)); GG(MM(3,2,X),if(DMA && MODE<2) DW(q,0); else if(DMA) FW8(q)); GG(MM(3,3,X),if(DMA && MODE<2) DW(q,1)); \
    GG(MS(0,X),if(DMA && MODE<2) DW8(q)); MS(1,X); \
    if(DMA){ if(MODE<2){ if((q)&1) gT+=128; gF+=32*FF*2; } else { gT+=8192; gF+=9216; } } SBAR(); }while(0)
#define RAWPHASE(PRE,POST) do{ \
    WAIT_VM(10); BARRIER(); RDALL(fa,2); CYSTART(); \
    STEP(fa,fb_,2,true,8); STEP(fb_,fa,3,true,4); \
    _Pragma("unroll 1") for(int j=0;j<3;j++){ STEP(fa,fb_,0,true,8); STEP(fb_,fa,1,true,4); STEP(fa,fb_,2,true,8); STEP(fb_,fa,3,true,4); } \
    PRE; STEP(fa,fb_,0,false,8); POST; STEP(fb_,fa,1,false,0); STEP(fa,fb_,2,false,0); STEP(fb_,fa,3,false,0); MFMA_DRAIN(); SBAR(); }while(0)

  TS(0,0);
  raw_start(FH+fb*144);
  acc_zero(acc);
  float csl[5]; f32x2 rowst[4][4], rst[4]; f32x4 cs1[5];
  auto ld_stats=[&](){
#pragma unroll
    for(int fi=0;fi<5;fi++) csl[fi]=ld4(ws+WS_CS+(FH+fb*144+(fi<4? FU(fi):8)*16+r)*4);
#pragma unroll
    for(int ti=0;ti<4;ti++){ const int s0=tb*128+(wt*4+((ti+2*wf)&3))*16;
#pragma unroll
      for(int e=0;e<4;e++) rowst[ti][e]=ld8(ws+WS_LN+(s0+4*g+e)*8);
      rst[ti]=ld8(ws+WS_LN+(s0+r)*8); } };
  { constexpr int MODE=0; RAWPHASE(if(wid==0) poll_stats(ws,lane,tag), ld_stats()); }
  CYDUMP(5);
  TS(0,1);
#pragma unroll
  for(int ti=0;ti<4;ti++){ PIN(rst[ti]); PIN(csl[ti]);
#pragma unroll
    for(int e=0;e<4;e++) PIN(rowst[ti][e]); }
  PIN(csl[4]);
  WAIT_LGKM0(); BARRIER();
  raw_start(fb*144);
  { char* z2f=ws+WS_Z2F+(long)fb*32*FST1; char* stp=ws+WS_ST+(long)(fb*2+wf)*S*8;
    u32x2 ps[2];
#pragma unroll
    for(int ti=0;ti<4;ti++){
      const int tu=wt*4+((ti+2*wf)&3); const int s0=tb*128+tu*16;
      char* zb=z2f+(long)(s0>>5)*9216+((((s0>>4)&1)*2+(g>>1))*16+r)*16;
      const f32x2 mu01={rowst[ti][0].x,rowst[ti][1].x}, mu23={rowst[ti][2].x,rowst[ti][3].x}, rs01={rowst[ti][0].y,rowst[ti][1].y}, rs23={rowst[ti][2].y,rowst[ti][3].y};
      f32x2 z01[5], z23[5]; u32x2 pp[5];
#pragma unroll
      for(int fi=0;fi<5;fi++){ if(fi==4 && ti>=2) break;
        const f32x4 a= fi<4? acc.m[fi][ti] : acc.s[ti&1];
        f32x2 y0=gelu2(((f32x2){a[0],a[1]}-mu01*csl[fi])*rs01), y1=gelu2(((f32x2){a[2],a[3]}-mu23*csl[fi])*rs23);
        u32x2 p={cvt2(y0.x,y0.y),cvt2(y1.x,y1.y)}; pp[fi]=p;
        z01[fi]=(f32x2){bflo(p[0]),bfhi(p[0])}; z23[fi]=(f32x2){bflo(p[1]),bfhi(p[1])}; }
      st16(zb+(FU(0)+(g&1))*1024,pair16(pp[0],pp[1])); st16(zb+(FU(2)+(g&1))*1024,pair16(pp[2],pp[3]));
      if(ti<2) ps[ti]=pp[4];
      if(ti==1){ const int sA=tb*128+(wt*4+2*wf)*16;
        st16(z2f+(long)(sA>>5)*9216+8*1024+(((g&1)*2+(g>>1))*16+r)*16,pair16(ps[0],ps[1])); }
      const int nf= ti<2? 5:4; const float inv=1.0f/(16*nf);
      f32x2 sa=(z01[0]+z01[1])+(z01[2]+z01[3]), sb=(z23[0]+z23[1])+(z23[2]+z23[3]); if(ti<2){ sa+=z01[4]; sb+=z23[4]; }
      f32x2 ma={row16_sum(sa.x)*inv,row16_sum(sa.y)*inv}, mb={row16_sum(sb.x)*inv,row16_sum(sb.y)*inv};
      f32x2 qa={0,0}, qb={0,0};
#pragma unroll
      for(int fi=0;fi<5;fi++) if(fi<nf){ f32x2 d=z01[fi]-ma, e=z23[fi]-mb; qa+=d*d; qb+=e*e; }
      const float q0=row16_sum(qa.x), q1=row16_sum(qa.y), q2=row16_sum(qb.x), q3=row16_sum(qb.y);
      const f32x4 res= r==0? (f32x4){ma.x,q0,ma.y,q1} : (f32x4){mb.x,q2,mb.y,q3};
      if(r<2) st16(stp+(long)(s0+4*g+2*r)*8,__builtin_bit_cast(u32x4,res));
    } }
  acc_zero(acc);
  WAIT_VM0(); BARRIER();
  if(threadIdx.x==0) set_flag(ws,id,FS,tag);
  TS(0,2);
  auto ld_cs1=[&](){
#pragma unroll
    for(int fi=0;fi<5;fi++)
#pragma unroll
      for(int e=0;e<4;e++) cs1[fi][e]=ld4(ws+WS_CS+(fb*144+(fi<4? FU(fi):8)*16+4*g+e)*4); };
  { constexpr int MODE=1; RAWPHASE((void)0, ld_cs1()); }
  CYDUMP(6);
  TS(0,3);
  if(wid==0){ const int pid= lane<16? tile_id(lane,tb) : tile_id(lane&15,fb); wait_flags(ws,pid,lane<16? FW:FS,tag); }
  unsigned z1m[4][4][2], z1s[2][2]; float bb[4];
#pragma unroll
  for(int fi=0;fi<5;fi++){ const f32x4 c4=cs1[fi];
#pragma unroll
    for(int ti=0;ti<4;ti++){ if(fi==4 && ti>=2) break;
      const f32x4 a= fi<4? acc.m[fi][ti] : acc.s[ti&1];
      f32x2 y0=gelu2(((f32x2){a[0],a[1]}-(f32x2){c4[0],c4[1]}*rst[ti].x)*rst[ti].y), y1=gelu2(((f32x2){a[2],a[3]}-(f32x2){c4[2],c4[3]}*rst[ti].x)*rst[ti].y);
      unsigned w0=cvt2(y0.x,y0.y), w1=cvt2(y1.x,y1.y); PIN(w0); PIN(w1);
      if(fi<4){ z1m[fi][ti][0]=w0; z1m[fi][ti][1]=w1; } else { z1s[ti&1][0]=w0; z1s[ti&1][1]=w1; } } }
  acc_zero(acc);
  WAIT_VM0(); WAIT_LGKM0(); BARRIER();
  TS(0,4);
  gT=ws+WS_WPF+(long)tb*32*TST+wid*2048; gF=ws+WS_Z2F+(long)fb*32*FST1;
  FO_POS(0); FO_POS(1); FO_POS(2); FO_POS(3);
  f32x4 cpv[4]; u16 bsv[4];
#pragma unroll
  for(int ti=0;ti<4;ti++){ const int t=tb*128+(wt*4+((ti+2*wf)&3))*16+r; const float* cp=(const float*)(ws+WS_CP)+t+g*4*S;
    cpv[ti]=(f32x4){cp[0],cp[S],cp[2*S],cp[3*S]}; bsv[ti]=in.b_sp[t]; }
  TS(0,5);
  { constexpr int MODE=2;
    WAIT_VM(12); BARRIER(); RDALL(fa,0); CYSTART();
#pragma unroll 1
    for(int j=0;j<15;j++){ STEP(fa,fb_,0,true,8); STEP(fb_,fa,1,true,8); STEP(fa,fb_,2,true,8); STEP(fb_,fa,3,true,8); }
    STEP(fa,fb_,0,false,8); STEP(fb_,fa,1,false,4); STEP(fa,fb_,2,false,0); STEP(fb_,fa,3,false,0); MFMA_DRAIN(); SBAR(); }
  CYDUMP(7);
  TS(0,6);
#pragma unroll
  for(int ti=0;ti<4;ti++) bb[ti]=bflo(bsv[ti])-gsum((cpv[ti][0]+cpv[ti][1])+(cpv[ti][2]+cpv[ti][3]));
  { char* hf=ws+WS_HF+(long)tb*45*TST;
    auto hv=[&](const f32x4& a,unsigned z0,unsigned z1,float bv){
      return (u32x2){cvt2(bflo(z0)*(a[0]+bv),bfhi(z0)*(a[1]+bv)),cvt2(bflo(z1)*(a[2]+bv),bfhi(z1)*(a[3]+bv))}; };
    auto cell=[&](int tu,int f16){ const int f=f16+(g>>1)*8; return hf+(long)(f>>5)*8192+tu*1024+(((f>>3)&3)*16+r)*16; };
#pragma unroll
    for(int ti=0;ti<4;ti++){ const int tu=wt*4+((ti+2*wf)&3);
#pragma unroll
      for(int fi=0;fi<4;fi+=2)
        st16(cell(tu,fb*144+(FU(fi)+(g&1))*16),pair16(hv(acc.m[fi][ti],z1m[fi][ti][0],z1m[fi][ti][1],bb[ti]),hv(acc.m[fi+1][ti],z1m[fi+1][ti][0],z1m[fi+1][ti][1],bb[ti]))); }
    st16(cell(wt*4+2*wf+(g&1),fb*144+128),pair16(hv(acc.s[0],z1s[0][0],z1s[0][1],bb[0]),hv(acc.s[1],z1s[1][0],z1s[1][1],bb[1]))); }
  WAIT_VM0(); WAIT_LGKM0(); BARRIER();
  if(threadIdx.x==0) set_flag(ws,id,FHH,tag);
  TS(0,7);
  { const int th=fb&1, jc=fb>>1, nf=4+(jc&1), du0=(jc>>1)*9+(jc&1)*4, kk=wid>>1;
    if(wid==0){ poll_flags(ws,lane,FW,tag); poll_flags(ws,64+lane,FW,tag); wait_flags(ws,tile_id(tb,lane&15),FHH,tag); }
    WAIT_VM0(); BARRIER();
    TS(1,0);
    const char* gT4=ws+WS_HF+(long)tb*45*TST+(kk*8+th*4+wt*2)*1024; const char* gF4=ws+WS_WOF+((long)kk*36+du0+wt*2)*1024;
    const unsigned lT4=lds0+(kk*4+wt*2)*1024, lF4=lds0+8192+kk*5120+wt*2048; const int x5=(jc&1)+2*wt;
#define D4(q) do{ glds2(gT4,vF,lT4+(q)*18432); glds2(gF4,vF,lF4+(q)*18432); eglds<1>(x5,gF4+4096,vF,lF4+4096+(q)*18432); gT4+=TST; gF4+=72*1024; }while(0)
    f32x4 a4[5][2];
#pragma unroll
    for(int n=0;n<5;n++){ a4[n][0]=(f32x4){0,0,0,0}; a4[n][1]=(f32x4){0,0,0,0}; PIN(a4[n][0]); PIN(a4[n][1]); }
    const LAS char* t4=L+(kk*4+wt*2)*1024+lane*16; const LAS char* f4=L+8192+kk*5120+lane*16;
    struct F4 { u32x4 t0,t1,f[5]; } xa,xb;
#define R4(X,q) do{ X.t0=LD(t4+(q)*18432); X.f[0]=LD(f4+(q)*18432); X.t1=LD(t4+(q)*18432+1024); _Pragma("unroll") for(int n=1;n<5;n++) X.f[n]=LD(f4+(q)*18432+n*1024); }while(0)
#define S4(X,Y,q,DMA,VM) do{ SBAR(); WAIT_LGKM0(); MF16A(a4[0][0],X.f[0],X.t0); MF16A(a4[0][1],X.f[0],X.t1); WAIT_VM(VM); BARRIER(); SBAR(); \
      R4(Y,((q)+1)&3); SBAR(); if(DMA) D4(q); SBAR(); \
      _Pragma("unroll") for(int n=1;n<5;n++){ MF16A(a4[n][0],X.f[n],X.t0); MF16A(a4[n][1],X.f[n],X.t1); } SBAR(); }while(0)
    D4(0); D4(1); D4(2); D4(3);
    WAIT_VM(12); BARRIER(); R4(xa,0);
#pragma unroll 1
    for(int j=0;j<8;j++){ S4(xa,xb,0,true,8); S4(xb,xa,1,true,8); S4(xa,xb,2,true,8); S4(xb,xa,3,true,8); }
    S4(xa,xb,0,false,8); S4(xb,xa,1,false,4); S4(xa,xb,2,false,0); S4(xb,xa,3,false,0);
    TS(1,1);
    if(wid==0) wait_flags(ws,tile_id(tb,lane&15),FO,tag);
    WAIT_VM0(); WAIT_LGKM0(); BARRIER();
    TS(1,2);
    D4(3); D4(0); D4(1); D4(2);
    WAIT_VM(12); BARRIER(); R4(xb,3);
    S4(xb,xa,3,true,8); S4(xa,xb,0,true,8); S4(xb,xa,1,true,8); S4(xa,xb,2,true,8); S4(xb,xa,3,true,8);
    S4(xa,xb,0,false,8); S4(xb,xa,1,false,4); S4(xa,xb,2,false,0); S4(xb,xa,3,false,0);
    MFMA_DRAIN(); WAIT_LGKM0(); BARRIER();
    f32x4* ex=(f32x4*)lds+wt*640+lane;
    if(kk==1){
#pragma unroll
      for(int n=0;n<5;n++){ ex[(n*2)*64]=a4[n][0]; ex[(n*2+1)*64]=a4[n][1]; } }
    WAIT_LGKM0(); BARRIER();
    if(kk==0){
#pragma unroll
      for(int ti=0;ti<2;ti++){ const long row=(long)(tb*128+th*64+(wt*2+ti)*16+r)*D+du0*16+4*g;
#pragma unroll
        for(int n=0;n<5;n++) if(n<nf){ const f32x4 a=a4[n][ti]+ex[(n*2+ti)*64]; const u32x2 xv=*(const u32x2*)(in.x+row+n*16);
          *(u32x2*)(out+row+n*16)=(u32x2){cvt2(bflo(xv[0])+a[0],bfhi(xv[0])+a[1]),cvt2(bflo(xv[1])+a[2],bfhi(xv[1])+a[3])}; } } }
    TS(1,3);
  }
}

#define PRIO_G 1
#define PRIO_A 2
#define PRIO_P 0
#define PRIO_T 0
DI float max3f(float a,float b,float c){ return __builtin_fmaxf(__builtin_fmaxf(a,b),c); }
DI float tmax16(const f32x16& s){
  float a=max3f(s[0],s[1],s[2]), b=max3f(s[3],s[4],s[5]); a=max3f(a,s[6],s[7]); b=max3f(b,s[8],s[9]); a=max3f(a,s[10],s[11]); b=max3f(b,s[12],s[13]); a=max3f(a,s[14],s[15]); return __builtin_fmaxf(a,b); }
DI float xhalf_max(float m){ auto rr=__builtin_amdgcn_permlane32_swap(__float_as_uint(m),__float_as_uint(m),false,false); return __builtin_fmaxf(__uint_as_float(rr[0]),__uint_as_float(rr[1])); }
#define EXP_NQ 8
#define ATT_BIAS 60.f
#define ATT_THR 1.8446744e19f
DI float set_m(unsigned& w,unsigned qw,float m,bool low){
  unsigned hi=cvt2(-m,0.f)&0xffffu; float fh=__uint_as_float(hi<<16); float rem=-m-fh; unsigned lo=cvt2(rem,0.f)&0xffffu; float fl=__uint_as_float(lo<<16);
  w= low? qw : (hi|(lo<<16)); return -(fh+fl); }
DI void transpose8x8(const u32x4 (&in)[8],u32x4 (&out)[8]){
#pragma unroll
  for(int n2=0;n2<4;n2++)
#pragma unroll
    for(int k2=0;k2<4;k2++){ unsigned a=in[2*k2][n2], b=in[2*k2+1][n2];
      out[2*n2][k2]=__builtin_amdgcn_perm(b,a,0x05040100u); out[2*n2+1][k2]=__builtin_amdgcn_perm(b,a,0x07060302u); } }
constexpr int A_SC=8*KVCH;

template<int KIND>
DI void a_proj(const In& in,char* ws,char* lds,int tb,int fb,int wid,int lane,unsigned tag){
  const int r=lane&15, g=lane>>4, lr=lane>>3;
  const LAS char* L=(const LAS char*)lds; const unsigned lds0=(unsigned)(size_t)lds;
  constexpr int NT= KIND==0? 3:2, FO_=NT*16384, NU= KIND==0? 3:2;
  unsigned vt[4], vf[2];
#pragma unroll
  for(int i=0;i<4;i++){ const int ii=wid*4+i; vt[i]=(8*ii+lr)*1152+(((lane&7)^(((lr>>1)+4*ii)&7))*16); }
  const int fp0= wid<2? wid*2 : 2+wid, nfp= wid<2? 2:1;
#pragma unroll
  for(int i=0;i<2;i++){ int c=(fp0+(i<nfp? i:0))*64+lane; int slot=c/6, ch=c-slot*6; vf[i]=swap23(slot)*1152+ch*16; }
  const char* gt=(const char*)(in.x+(long)tb*128*D); const char* gf=(const char*)(in.wq+fb*48);
  auto stage=[&](int s){ const unsigned mt=lds0+(s%NT)*16384+wid*4096;
#pragma unroll
    for(int i=0;i<4;i++) glds(gt+s*128,vt[i],mt+i*1024);
    if(KIND==0){ const unsigned mf=lds0+FO_+(s%NT)*6144+fp0*1024; glds(gf+(long)s*64*1152,vf[0],mf); cglds<1>(nfp,gf+(long)s*64*1152,vf[1],mf+1024); } };
#pragma unroll
  for(int s=0;s<NT-1;s++) stage(s);
  if(KIND>0){
    const u16* w= KIND==1? in.wk:in.wv;
#pragma unroll 1
    for(int cell=threadIdx.x; cell<72*18; cell+=256){ const int ko=cell/18, col=cell-ko*18; u32x4 v;
#pragma unroll
      for(int j=0;j<4;j++) v[j]=(unsigned)w[(ko*8+2*j)*18+col] | ((unsigned)w[(ko*8+2*j+1)*18+col]<<16);
      *(LAS u32x4*)((LAS char*)lds+FO_+((ko>>2)*2+(col>>4))*1024+((ko&3)*16+(col&15))*16)=v; } }
  f32x4 acc[3][2];
#pragma unroll
  for(int n=0;n<3;n++){ acc[n][0]=(f32x4){0,0,0,0}; acc[n][1]=(f32x4){0,0,0,0}; }
  const int sl=(r>>2)+4*(g&1)+16*(g>>1);
#pragma unroll 1
  for(int s=0;s<9;s++){
    WAIT_LGKM0();
    if(NT==3 && s<8){ if(wid<2) asm volatile("s_waitcnt vmcnt(6)":::"memory"); else asm volatile("s_waitcnt vmcnt(5)":::"memory"); } else WAIT_VM0();
    BARRIER();
    if(s+NT-1<9) stage(s+NT-1);
    const LAS char* T=L+(s%NT)*16384+(wid*32+r)*128;
#pragma unroll
    for(int k=0;k<2;k++){
      const int ch=((4*k+g)^((r>>1)&7))*16;
      u32x4 t0=LD(T+ch), t1=LD(T+2048+ch), f[3];
#pragma unroll
      for(int n=0;n<NU;n++){
        if(KIND==0){ const LAS char* p=L+FO_+(s%NT)*6144+(sl+32*k)*96+(lane&3)*8+n*32; u32x2 lo=trd(p), hi=trd(p+8*96); f[n]=(u32x4){lo[0],lo[1],hi[0],hi[1]}; }
        else f[n]=LD(L+FO_+((s*2+k)*2+n)*1024+lane*16); }
#pragma unroll
      for(int n=0;n<NU;n++){
        if(KIND==2){ acc[n][0]=MF16(t0,f[n],acc[n][0]); acc[n][1]=MF16(t1,f[n],acc[n][1]); }
        else { acc[n][0]=MF16(f[n],t0,acc[n][0]); acc[n][1]=MF16(f[n],t1,acc[n][1]); } } }
  }
  if(wid==0) poll_stats(ws,lane,tag);
  BARRIER();
  const char* csb=ws+WS_CS+(KIND==0? FF+fb*48 : (KIND==1? FF+D : FF+D+32))*4; const char* lnb=ws+WS_LN+(long)(tb*128+wid*32)*8;
  char* kv=ws+WS_KV+(long)tb*KVCH;
  if(KIND<2){ f32x2 st[2]; f32x4 c4[3];
    st[0]=ld8(lnb+r*8); st[1]=ld8(lnb+(16+r)*8);
#pragma unroll
    for(int n=0;n<NU;n++)
#pragma unroll
      for(int e=0;e<4;e++) c4[n][e]=ld4(csb+(n*16+4*g+e)*4);
    if(KIND==0){ char* q=ws+WS_Q+((long)(tb*128+wid*32+(g&1)*16+r)*D+fb*48+(g>>1)*8)*2;
#pragma unroll
      for(int n=0;n<3;n++){ const f32x4 a=(acc[n][0]-c4[n]*st[0].x)*(st[0].y*QSCALE), b=(acc[n][1]-c4[n]*st[1].x)*(st[1].y*QSCALE);
        st16(q+n*32,pair16((u32x2){cvt2(a[0],a[1]),cvt2(a[2],a[3])},(u32x2){cvt2(b[0],b[1]),cvt2(b[2],b[3])})); } }
    else {
#pragma unroll
      for(int ti=0;ti<2;ti++){ const int rr=ti*16+r;
        const f32x4 a=(acc[0][ti]-c4[0]*st[ti].x)*st[ti].y, c=(acc[1][ti]-c4[1]*st[ti].x)*st[ti].y;
        st8(kv+KV_K0+wid*1024+((g>>1)*32+rr)*16+(g&1)*8,(u32x2){cvt2(a[0],a[1]),cvt2(a[2],a[3])});
        if(g==0) st4(kv+KV_K1+rr*16+wid*4,cvt2(c[0],c[1]));
        if(g==1) st4(kv+KV_K1+512+rr*16+wid*4,ONES); } } }
  else { const float c0=ld4(csb+r*4), c1=ld4(csb+(16+r)*4);
#pragma unroll
    for(int ti=0;ti<2;ti++){ char* vb=kv+KV_V+wid*VT+ti*608+(g&1)*304+(g>>1)*8; f32x4 mu,rs;
#pragma unroll
      for(int e=0;e<4;e++){ f32x2 p=ld8(lnb+(ti*16+4*g+e)*8); mu[e]=p.x; rs[e]=p.y; }
      const f32x4 a=(acc[0][ti]-mu*c0)*rs, c=(acc[1][ti]-mu*c1)*rs;
      st8(vb+r*16,(u32x2){cvt2(a[0],a[1]),cvt2(a[2],a[3])});
      if(r<2) st8(vb+(16+r)*16,(u32x2){cvt2(c[0],c[1]),cvt2(c[2],c[3])});
      if(r==2) st8(vb+18*16,(u32x2){ONES,ONES}); } }
}

DI void a_colsum(const In& in,char* ws,char* lds,int c,int wid,int lane){
  const int tid=wid*64+lane; float* red=(float*)lds;
  if(c<81){ const int c8=tid&7, rg=tid>>3; const bool q=c>=72; const int ld= q? D:FF;
    const u16* p=(q? in.wq+(c-72)*64 : in.w_in+c*64)+(long)rg*ld+c8*8; u32x4 v[18];
#pragma unroll
    for(int i=0;i<18;i++) v[i]=*(const u32x4*)(p+(long)i*32*ld);
    f32x2 a[4]={{0,0},{0,0},{0,0},{0,0}};
#pragma unroll
    for(int i=0;i<18;i++)
#pragma unroll
      for(int e=0;e<4;e++) a[e]+=(f32x2){bflo(v[i][e]),bfhi(v[i][e])};
#pragma unroll
    for(int e=0;e<4;e++) *(f32x2*)(red+rg*64+c8*8+2*e)=a[e];
    WAIT_LGKM0(); BARRIER();
    if(tid<64){ float s=0;
#pragma unroll
      for(int i=0;i<32;i++) s+=red[i*64+tid];
      st4(ws+WS_CS+(c*64+tid)*4,__float_as_uint(s)); } }
  else { const int rg=tid/36, o=tid-rg*36; float s=0;
    if(rg<7){ const u16* p=(o<18? in.wk+o : in.wv+(o-18)); u16 v[83];
#pragma unroll
      for(int i=0;i<83;i++){ const int k=rg+7*i; v[i]=p[(k<D? k:0)*18]; }
#pragma unroll
      for(int i=0;i<83;i++) if(rg+7*i<D) s+=bflo(v[i]);
      red[rg*36+o]=s; }
    WAIT_LGKM0(); BARRIER();
    if(tid<36){ float t=0;
#pragma unroll
      for(int i=0;i<7;i++) t+=red[i*36+tid];
      st4(ws+WS_CS+(FF+D+(tid<18? tid : 32+tid-18))*4,__float_as_uint(t)); } }
}
DI void a_rowstat(const In& in,char* ws,int h,int wid,int lane){
  const int lr=lane>>3, c8=lane&7; u32x4 v[2][9];
#pragma unroll
  for(int i=0;i<2;i++)
#pragma unroll
    for(int s=0;s<9;s++) v[i][s]=*(const u32x4*)(in.x+(long)(h*64+wid*16+i*8+lr)*D+c8*8+s*64);
  TS(4,0);
  TS(4,1);
#pragma unroll
  for(int i=0;i<2;i++){ const int row=h*64+wid*16+i*8+lr;
    f32x2 sm={0,0};
#pragma unroll
    for(int s=0;s<9;s++)
#pragma unroll
      for(int e=0;e<4;e++) sm+=(f32x2){bflo(v[i][s][e]),bfhi(v[i][s][e])};
    float t=sm.x+sm.y; DPPADD(t,0xB1); DPPADD(t,0x4E); DPPADD(t,0x141); const float mu=t*(1.0f/D);
    f32x2 q={0,0};
#pragma unroll
    for(int s=0;s<9;s++)
#pragma unroll
      for(int e=0;e<4;e++){ f32x2 d=(f32x2){bflo(v[i][s][e]),bfhi(v[i][s][e])}-mu; q+=d*d; }
    t=q.x+q.y; DPPADD(t,0xB1); DPPADD(t,0x4E); DPPADD(t,0x141);
    if(c8==0) st8(ws+WS_LN+(long)row*8,(u32x2){__float_as_uint(mu),__float_as_uint(1.0f/sqrtf(t*(1.0f/D)+1e-5f))}); }
}

DI void a_role(const In& in,char* ws,char* lds,int id,int aw,int lane,unsigned tag){
  int tb,fb; tile_map(id,tb,fb);
  TS(2,0);
  if(fb>=14 || fb*16+tb<82){
    if(fb>=14) a_rowstat(in,ws,tb*2+(fb-14),aw,lane); else a_colsum(in,ws,lds,fb*16+tb,aw,lane);
    TS(4,2); WAIT_VM0(); TS(4,3); WAIT_LGKM0(); BARRIER(); if(threadIdx.x==0) set_flag(ws,id,FL,tag); }
  TS(3,0);
  __builtin_amdgcn_s_setprio(PRIO_P);
  if(fb<12) a_proj<0>(in,ws,lds,tb,fb,aw,lane,tag); else if(fb==12) a_proj<1>(in,ws,lds,tb,fb,aw,lane,tag); else if(fb==13) a_proj<2>(in,ws,lds,tb,fb,aw,lane,tag);
  WAIT_VM0(); WAIT_LGKM0(); BARRIER();
  if(threadIdx.x==0) set_flag(ws,id,FQ,tag);
  TS(2,1);
  u32x4 wv_[2][4]; const int r16=lane&15, g=lane>>4;
  { const u16* wp=in.w_sp+(long)(fb*128+aw*32+r16)*S+tb*128+g*8;
#pragma unroll
    for(int u=0;u<2;u++)
#pragma unroll
      for(int k=0;k<4;k++) wv_[u][k]=*(const u32x4*)(wp+(long)u*16*S+k*32); }
  { const int task=id*4+aw;
    if(task<450){ const int k32=task/5, cb=task-k32*5, c=cb*128+(lane&15)*8;
      const u16* src= k32<72? in.w_out+(long)k32*32*D : in.wo+(long)(k32-72)*32*D;
      u32x4 rin[8], rout[8]; const int cc= c<D? c:0;
#pragma unroll
      for(int j=0;j<8;j++) rin[j]=*(const u32x4*)(src+(long)(g*8+j)*D+cc);
      transpose8x8(rin,rout);
      LAS char* bw=(LAS char*)lds+aw*8192;
#pragma unroll
      for(int n=0;n<8;n++) *(LAS u32x4*)(bw+((lane&15)>>1)*1024+g*256+((lane&1)*8+n)*16)=rout[n];
      WAIT_LGKM0(); asm volatile("":::"memory");
      char* dst=ws+WS_WOF+((long)k32*36+cb*8)*1024+lane*16; const int nu= cb<4? 8:4;
#pragma unroll
      for(int u=0;u<8;u++){ u32x4 v=LD(bw+u*1024+lane*16); if(u<nu) st16(dst+u*1024,v); }
      WAIT_LGKM0(); } }
  __builtin_amdgcn_s_setprio(PRIO_A);
  if(aw==0) poll_flags(ws,tile_id(tb,lane&15),FS,tag);
  BARRIER();
  TS(2,2);
  float* sc=(float*)(lds+A_SC)+64;
  { const int tid=aw*64+lane, sl=tid>>1, hf=tid&1; const int s=tb*128+sl;
    const char* st=ws+WS_ST+((long)hf*16*S+s)*8;
    f32x2 v[16];
#pragma unroll
    for(int i=0;i<16;i++) v[i]=ld8(st+(long)i*S*8);
    const int big=((s>>4)&3)>>1;
    float sm=0;
#pragma unroll
    for(int i=0;i<16;i++) sm+=v[i].x*(((i&1)==big)? 80.f:64.f);
    sm+=__shfl_xor(sm,1); const float mu=sm*(1.0f/FH); float M2=0;
#pragma unroll
    for(int i=0;i<16;i++){ const float d=v[i].x-mu; M2+=v[i].y+d*d*(((i&1)==big)? 80.f:64.f); }
    M2+=__shfl_xor(M2,1);
    if(hf==0){ sc[sl]=1.0f/sqrtf(M2*(1.0f/FH)+1e-5f); sc[128+sl]=mu; } }
  TS(4,4);
  WAIT_LGKM0(); BARRIER();
  { char* wpf=ws+WS_WPF+((long)fb*32+tb*2)*TST+aw*2048+lane*16;
#pragma unroll
    for(int u=0;u<2;u++){ float cp=0;
#pragma unroll
      for(int k=0;k<4;k++){ const f32x4* rs=(const f32x4*)(sc+k*32+g*8); const f32x4* mu=(const f32x4*)(sc+128+k*32+g*8);
        f32x4 r0=rs[0],r1=rs[1],m0=mu[0],m1=mu[1]; u32x4 o; const u32x4 w=wv_[u][k];
        o[0]=cvt2(bflo(w[0])*r0[0],bfhi(w[0])*r0[1]); o[1]=cvt2(bflo(w[1])*r0[2],bfhi(w[1])*r0[3]);
        o[2]=cvt2(bflo(w[2])*r1[0],bfhi(w[2])*r1[1]); o[3]=cvt2(bflo(w[3])*r1[2],bfhi(w[3])*r1[3]);
        cp+=bflo(o[0])*m0[0]+bfhi(o[0])*m0[1]+bflo(o[1])*m0[2]+bfhi(o[1])*m0[3]+bflo(o[2])*m1[0]+bfhi(o[2])*m1[1]+bflo(o[3])*m1[2]+bfhi(o[3])*m1[3];
        st16(wpf+(long)k*8192+u*1024,o); }
      cp=gsum(cp);
      if(lane<16) st4(ws+WS_CP+((long)tb*S+fb*128+aw*32+u*16+lane)*4,__float_as_uint(cp)); } }
  TS(4,5);
  TS(4,6);
  if(aw==0) wait_flags(ws,lane<16? tile_id(tb,lane<12? lane:0) : tile_id(lane&15,12+((lane>>4)&1)),FQ,tag);
  WAIT_VM0(); BARRIER();
  if(threadIdx.x==0) set_flag(ws,id,FW,tag);
  TS(2,3);
  const int r=lane&31, hh=lane>>5; const bool low=(hh==0);
  const LAS char* L=(const LAS char*)lds; const unsigned lb=(unsigned)(size_t)lds, vF=lane*16;
  auto stageKV=[&](int q4){ const char* gsrc=ws+WS_KV+(long)q4*2*KVCH; const unsigned m=lb+(q4&3)*2*KVCH;
#pragma unroll
    for(int i=0;i<5;i++){ int off=(aw*5+i)*1024; if(off>2*KVCH-1024) off=2*KVCH-1024; glds(gsrc+off,vF,m+off); } };
  const int sa=tb*128+fb*8+aw*2;
  u32x4 qa0,qb0; unsigned qaw,qbw;
  { const char* pa=ws+WS_Q+(long)sa*1152+r*36; const char* pb=pa+1152;
    typedef u32x4 __attribute__((aligned(4))) u32x4u;
    qa0=*(const u32x4u*)(pa+hh*16); qb0=*(const u32x4u*)(pb+hh*16);
    qaw=*(const unsigned*)(pa+32); qbw=*(const unsigned*)(pb+32); }
  stageKV(0);
  asm volatile("s_waitcnt vmcnt(0)":"+v"(qa0),"+v"(qb0),"+v"(qaw),"+v"(qbw)::"memory");
  BARRIER();
  stageKV(1); stageKV(2); stageKV(3);
  const int k0off=KV_K0+lane*16, k1off=KV_K1+lane*16, voff=KV_V+hh*304+r*16;
  const f32x16 Z={0,0,0,0,0,0,0,0,0,0,0,0,0,0,0,0};
  unsigned wa= low? qaw:0u, wb= low? qbw:0u;
  float ma=0.f, mb=0.f;
  f32x16 oa0=Z,oa1=Z,ob0=Z,ob1=Z;
  u32x4 k1,ka,kb,va0,va1,vb0,vb1;
#define K1P(t) ((u32x2){k1[(t)&2],k1[((t)&2)+1]})
#define QE(w,t) (((t)&1)? (u32x2){0u,w} : (u32x2){w,0u})
#define QK1(S_,w,t) S_=MF32K8(K1P(t),QE(w,t),S_)
#define CHUNKMAX(B,xa,xb) do{ k1=LD(B+k1off); _Pragma("unroll") for(int t=0;t<4;t++){ ka=LD(B+k0off+t*1024); \
      f32x16 s_=MF32(ka,qa0,Z); QK1(s_,wa,t); xa=__builtin_fmaxf(xa,tmax16(s_)); \
      f32x16 u_=MF32(ka,qb0,Z); QK1(u_,wb,t); xb=__builtin_fmaxf(xb,tmax16(u_)); } xa=xhalf_max(xa); xb=xhalf_max(xb); }while(0)
  { float xa=-3.0e38f, xb=-3.0e38f; CHUNKMAX(L,xa,xb); ma=set_m(wa,qaw,xa+ATT_BIAS,low); mb=set_m(wb,qbw,xb+ATT_BIAS,low); }
  TS(3,1);
  __builtin_amdgcn_s_setprio(PRIO_T);
#define EXP4(S_,i) do{ S_[i]=__builtin_amdgcn_exp2f(S_[i]); S_[i+1]=__builtin_amdgcn_exp2f(S_[i+1]); S_[i+2]=__builtin_amdgcn_exp2f(S_[i+2]); S_[i+3]=__builtin_amdgcn_exp2f(S_[i+3]); }while(0)
#define GRP(MF,S_,i,P,j) do{ MF; SBAR(); EXP4(S_,i); unsigned w0_=cvt2(S_[i],S_[i+1]), w1_=cvt2(S_[i+2],S_[i+3]); asm volatile("":"+v"(w0_),"+v"(w1_)); P[j]=w0_; P[j+1]=w1_; SBAR(); }while(0)
#define CHUNK(B,IA,IB,OA,OB) do{ \
    f32x16 sa_,sb_; u32x4 pa0,pa1,pb0,pb1; \
    ka=LD(B+k0off); k1=LD(B+k1off); kb=LD(B+k0off+1024); va0=LD(B+voff); va1=LD(B+voff+608); \
    sa_=MF32(ka,qa0,Z); QK1(sa_,wa,0); SBAR(); \
    _Pragma("unroll") for(int t=0;t<4;t+=2){ \
      GRP(sb_=MF32(ka,qb0,Z),sa_,0,pa0,0); \
      GRP(QK1(sb_,wb,t),sa_,4,pa0,2); \
      if(t>0){ GRP(OB=MF32(vb0,pb0,OB),sa_,8,pa1,0); GRP(OB=MF32(vb1,pb1,OB),sa_,12,pa1,2); } \
      else   { GRP((void)0,sa_,8,pa1,0); GRP((void)0,sa_,12,pa1,2); } \
      vb0=LD(B+voff+(t+1)*VT); vb1=LD(B+voff+(t+1)*VT+608); if(t+2<4) ka=LD(B+k0off+(t+2)*1024); SBAR(); \
      GRP(sa_=MF32(kb,qa0,Z),sb_,0,pb0,0); \
      GRP(QK1(sa_,wa,t+1),sb_,4,pb0,2); \
      if(t==0){ GRP(OA=MF32(va0,pa0,IA),sb_,8,pb1,0); } else { GRP(OA=MF32(va0,pa0,OA),sb_,8,pb1,0); } \
      GRP(OA=MF32(va1,pa1,OA),sb_,12,pb1,2); \
      GRP(sb_=MF32(kb,qb0,Z),sa_,0,pa0,0); \
      GRP(QK1(sb_,wb,t+1),sa_,4,pa0,2); \
      if(t==0){ GRP(OB=MF32(va0,pb0,IB),sa_,8,pa1,0); } else { GRP(OB=MF32(va0,pb0,OB),sa_,8,pa1,0); } \
      GRP(OB=MF32(va1,pb1,OB),sa_,12,pa1,2); \
      if(t+2<4){ va0=LD(B+voff+(t+2)*VT); va1=LD(B+voff+(t+2)*VT+608); kb=LD(B+k0off+(t+3)*1024); } SBAR(); \
      if(t+2<4){ GRP(sa_=MF32(ka,qa0,Z),sb_,0,pb0,0); GRP(QK1(sa_,wa,t+2),sb_,4,pb0,2); } \
      else     { GRP((void)0,sb_,0,pb0,0); GRP((void)0,sb_,4,pb0,2); } \
      GRP(OA=MF32(vb0,pa0,OA),sb_,8,pb1,0); \
      GRP(OA=MF32(vb1,pa1,OA),sb_,12,pb1,2); \
    } \
    OB=MF32(vb0,pb0,OB); OB=MF32(vb1,pb1,OB); \
  }while(0)
#define RESCALE(B,IA,IB) do{ float xa=-ATT_BIAS, xb=-ATT_BIAS; CHUNKMAX(B,xa,xb); \
    float na=set_m(wa,qaw,ma+xa+ATT_BIAS,low), nb=set_m(wb,qbw,mb+xb+ATT_BIAS,low); float fa_=__builtin_amdgcn_exp2f(ma-na), fb_=__builtin_amdgcn_exp2f(mb-nb); ma=na; mb=nb; \
    _Pragma("unroll") for(int i=0;i<16;i++){ IA[i]*=fa_; IB[i]*=fb_; } }while(0)
#define BAD(OA,OB) (__any( low && !((OA[10]<=ATT_THR) && (OB[10]<=ATT_THR)) ))
#pragma unroll 1
  for(int q4=0;q4<EXP_NQ;q4++){
    { const LAS char* B=L+(q4&3)*2*KVCH;
      for(int att=0;;att++){ CHUNK(B,oa0,ob0,oa1,ob1);
        if(__builtin_expect(att>0 || !BAD(oa1,ob1),1)) break; RESCALE(B,oa0,ob0); } }
    { const LAS char* B=L+(q4&3)*2*KVCH+KVCH;
      for(int att=0;;att++){ CHUNK(B,oa1,ob1,oa0,ob0);
        if(__builtin_expect(att>0 || !BAD(oa0,ob0),1)) break; RESCALE(B,oa1,ob1); } }
    if(q4<5){
      WAIT_VM0(); WAIT_LGKM0(); BARRIER();
      if(q4<4) stageKV(q4+4); }
  }
  TS(2,4);
  __builtin_amdgcn_s_setprio(PRIO_A);
  { auto ra=__builtin_amdgcn_permlane32_swap(__float_as_uint(oa0[10]),__float_as_uint(ob0[10]),false,false);
    auto rb=__builtin_amdgcn_permlane32_swap(__float_as_uint(ob0[10]),__float_as_uint(oa0[10]),false,false);
    const float x0=__uint_as_float(ra[0]), x1=__uint_as_float(rb[0]);
    const float ia=1.0f/(low? x0:x1), ib=1.0f/(low? x1:x0);
    WAIT_LGKM0(); BARRIER();
    LAS char* oa=(LAS char*)lds+(aw*2)*1152+r*36+hh*8; LAS char* ob=oa+1152;
    *(LAS unsigned*)(oa)=cvt2(oa0[0]*ia,oa0[1]*ia); *(LAS unsigned*)(oa+4)=cvt2(oa0[2]*ia,oa0[3]*ia); *(LAS unsigned*)(oa+16)=cvt2(oa0[4]*ia,oa0[5]*ia); *(LAS unsigned*)(oa+20)=cvt2(oa0[6]*ia,oa0[7]*ia);
    *(LAS unsigned*)(ob)=cvt2(ob0[0]*ib,ob0[1]*ib); *(LAS unsigned*)(ob+4)=cvt2(ob0[2]*ib,ob0[3]*ib); *(LAS unsigned*)(ob+16)=cvt2(ob0[4]*ib,ob0[5]*ib); *(LAS unsigned*)(ob+20)=cvt2(ob0[6]*ib,ob0[7]*ib);
    if(low){ *(LAS unsigned*)(oa+32)=cvt2(oa0[8]*ia,oa0[9]*ia); *(LAS unsigned*)(ob+32)=cvt2(ob0[8]*ib,ob0[9]*ib); }
    WAIT_LGKM0(); BARRIER();
    char* hf=ws+WS_HF+(long)tb*45*TST+(long)72*8192+(fb>>1)*1024+(fb&1)*128;
#pragma unroll
    for(int it=0;it<3;it++){ const int c=it*256+threadIdx.x; if(c<576){ const int i=c&7, ko=c>>3;
        st16(hf+(long)(ko>>2)*8192+(ko&3)*256+i*16,LD(L+i*1152+ko*16)); } }
  }
  WAIT_VM0(); BARRIER();
  if(threadIdx.x==0) set_flag(ws,id,FO,tag);
  TS(2,5);
}

__global__ __launch_bounds__(256,2) void mega(In in,char* __restrict__ ws,u16* __restrict__ out){
  extern __shared__ __attribute__((aligned(16))) char lds[];
  const int wid=__builtin_amdgcn_readfirstlane(threadIdx.x>>6), lane=threadIdx.x&63;
  const unsigned tag=1u;
  if(blockIdx.x<256){ __builtin_amdgcn_s_setprio(PRIO_G); g_role(in,ws,out,lds,blockIdx.x,wid,lane,tag); }
  else { __builtin_amdgcn_s_setprio(PRIO_A); a_role(in,ws,lds,blockIdx.x-256,wid,lane,tag); }
}

extern "C" void kernel_launch(void* const* d_in,const int* in_sizes,int n_in,void* d_out,int out_size,void* d_ws,size_t ws_size,hipStream_t stream){
  In in; in.x=(const u16*)d_in[0]; in.wq=(const u16*)d_in[1]; in.wk=(const u16*)d_in[2]; in.wv=(const u16*)d_in[3]; in.wo=(const u16*)d_in[4];
  in.w_in=(const u16*)d_in[6]; in.w_sp=(const u16*)d_in[7]; in.b_sp=(const u16*)d_in[8]; in.w_out=(const u16*)d_in[9];
  char* ws=(char*)d_ws;
  (void)hipFuncSetAttribute((const void*)mega,hipFuncAttributeMaxDynamicSharedMemorySize,LDS_BYTES);
  (void)hipMemsetAsync(ws+WS_FLAG,0,16384,stream);
  mega<<<512,256,LDS_BYTES,stream>>>(in,ws,(u16*)d_out);
}
```

```cpp
#include <hip/hip_runtime.h>
#include <hip/hip_bf16.h>
#include <type_traits>

typedef unsigned short u16;
using bf16x8 = __attribute__((ext_vector_type(8))) short;
using bf16x4 = __attribute__((ext_vector_type(4))) short;
using f32x2 = __attribute__((ext_vector_type(2))) float;
using f32x4 = __attribute__((ext_vector_type(4))) float;
using f32x16 = __attribute__((ext_vector_type(16))) float;
using u32x4 = __attribute__((ext_vector_type(4))) unsigned;
using u32x2 = __attribute__((ext_vector_type(2))) unsigned;
typedef __bf16 bf16x2_t __attribute__((ext_vector_type(2)));
typedef short v4i16_t __attribute__((ext_vector_type(4)));
typedef __attribute__((address_space(1))) unsigned gu32;
typedef __attribute__((address_space(1))) unsigned long long gu64;
#define LAS __attribute__((address_space(3)))
#define DI __device__ __forceinline__
#define SBAR() __builtin_amdgcn_sched_barrier(0)
#define MF32(a,b,c) __builtin_amdgcn_mfma_f32_32x32x16_bf16(__builtin_bit_cast(bf16x8,a),__builtin_bit_cast(bf16x8,b),c,0,0,0)
#define MF16(a,b,c) __builtin_amdgcn_mfma_f32_16x16x32_bf16(__builtin_bit_cast(bf16x8,a),__builtin_bit_cast(bf16x8,b),c,0,0,0)
#define MF32K8(a,b,c) __builtin_amdgcn_mfma_f32_32x32x8bf16_1k(__builtin_bit_cast(bf16x4,a),__builtin_bit_cast(bf16x4,b),c,0,0,0)
#define MF16A(c,a,b) asm volatile("v_mfma_f32_16x16x32_bf16 %0, %1, %2, %0" : "+v"(c) : "v"(a),"v"(b))
#define MFMA_DRAIN() asm volatile("s_nop 15\n\ts_nop 3":::"memory")
#define LD(p) (*(const LAS u32x4*)(p))
#define WAIT_LGKM0() __builtin_amdgcn_s_waitcnt(0xC07F)
#define WAIT_VM0() asm volatile("s_waitcnt vmcnt(0)":::"memory")
#define PIN(x) asm volatile("":"+v"(x))
#define BARRIER() do{ asm volatile("":::"memory"); __builtin_amdgcn_s_barrier(); asm volatile("":::"memory"); }while(0)
#define RLX_AGENT __ATOMIC_RELAXED, __HIP_MEMORY_SCOPE_AGENT
#define ONES 0x3f803f80u

DI unsigned cvt2(float a,float b){ f32x2 v={a,b}; return __builtin_bit_cast(unsigned,__builtin_convertvector(v,bf16x2_t)); }
DI float bflo(unsigned v){ return __uint_as_float(v<<16); }
DI float bfhi(unsigned v){ return __uint_as_float(v&0xffff0000u); }
DI u32x2 trd(const LAS char* p){ return __builtin_bit_cast(u32x2,__builtin_amdgcn_ds_read_tr16_b64_v4i16((LAS v4i16_t*)p)); }
DI int swap23(int k){ return (k&~12)|((k&4)<<1)|((k&8)>>1); }
DI void glds(const char* sbase,unsigned voff,unsigned m0v){
  asm volatile("s_mov_b32 m0, %2\n\ts_nop 0\n\tglobal_load_lds_dwordx4 %1, %0 sc1" :: "s"(sbase),"v"(voff),"s"(m0v) : "memory"); }
template<int O> DI void gldo(const char* sbase,unsigned voff){
  asm volatile("global_load_lds_dwordx4 %1, %0 offset:%2 sc1" :: "s"(sbase),"v"(voff),"i"(O) : "memory"); }
DI void gldw(const char* sbase,unsigned voff,unsigned m0v){
  asm volatile("s_mov_b32 m0, %2\n\ts_nop 0\n\tglobal_load_lds_dword %1, %0 sc1" :: "s"(sbase),"v"(voff),"s"(m0v) : "memory"); }
DI void glds2(const char* sbase,unsigned voff,unsigned m0v){
  asm volatile("s_mov_b32 m0, %2\n\ts_nop 0\n\tglobal_load_lds_dwordx4 %1, %0 sc1\n\tglobal_load_lds_dwordx4 %1, %0 offset:1024 sc1" :: "s"(sbase),"v"(voff),"s"(m0v) : "memory"); }
template<int I> DI void eglds(int w,const char* sbase,unsigned voff,unsigned m0v){
  asm volatile("s_cmp_lg_u32 %3, %4\n\ts_cbranch_scc1 EG%=\n\ts_mov_b32 m0, %2\n\ts_nop 0\n\tglobal_load_lds_dwordx4 %1, %0 sc1\nEG%=:"
               :: "s"(sbase),"v"(voff),"s"(m0v),"s"(w),"i"(I) : "memory","scc"); }
#define WAIT_VM(n) asm volatile("s_waitcnt vmcnt(%0)"::"i"(n):"memory")
DI float ld4(const void* p){ return __uint_as_float(__hip_atomic_load((gu32*)p,RLX_AGENT)); }
DI u32x2 ld8u(const void* p){ unsigned long long v=__hip_atomic_load((gu64*)p,RLX_AGENT); return (u32x2){(unsigned)v,(unsigned)(v>>32)}; }
DI f32x2 ld8(const void* p){ unsigned long long v=__hip_atomic_load((gu64*)p,RLX_AGENT); return (f32x2){__uint_as_float((unsigned)v),__uint_as_float((unsigned)(v>>32))}; }
template<int I> DI void cglds(int n,const char* sbase,unsigned voff,unsigned m0v){
  asm volatile("s_cmp_le_i32 %3, %4\n\ts_cbranch_scc1 CG%=\n\ts_mov_b32 m0, %2\n\ts_nop 0\n\tglobal_load_lds_dwordx4 %1, %0 sc1\nCG%=:"
               :: "s"(sbase),"v"(voff),"s"(m0v),"s"(n),"i"(I) : "memory","scc"); }
DI void st16(void* p,u32x4 v){ asm volatile("global_store_dwordx4 %0, %1, off sc1\n\ts_nop 1" :: "v"(p),"v"(v) : "memory"); }
DI void st8(void* p,u32x2 v){ __hip_atomic_store((gu64*)p,(unsigned long long)v[0]|((unsigned long long)v[1]<<32),RLX_AGENT); }
DI void st4(void* p,unsigned v){ __hip_atomic_store((gu32*)p,v,RLX_AGENT); }

DI u32x4 pair16(u32x2 a,u32x2 b){
  auto s0=__builtin_amdgcn_permlane16_swap(a[0],b[0],false,false); auto s1=__builtin_amdgcn_permlane16_swap(a[1],b[1],false,false);
  return (u32x4){s0[0],s1[0],s0[1],s1[1]}; }

DI u32x4 pair32(u32x2 a,u32x2 b){
  auto s0=__builtin_amdgcn_permlane32_swap(a[0],b[0],false,false); auto s1=__builtin_amdgcn_permlane32_swap(a[1],b[1],false,false);
  return (u32x4){s0[0],s1[0],s0[1],s1[1]}; }

constexpr int S=2048, D=576, FH=2304, FF=4608;
constexpr int TST=16384, FST1=18432;
#define VH 320
constexpr int KV_K0=0, KV_K1=4096, KV_V=5120, VT=4*VH, KVCH=KV_V+4*VT, NCH=16;
constexpr float QSCALE=0.23570226039551584f*1.4426950408889634f;
constexpr int LDS_BYTES=81920;

constexpr size_t WS_FLAG=0;
constexpr size_t WS_LN=16384;
constexpr size_t WS_CS=WS_LN+16384;
constexpr size_t WS_WOF=WS_CS+32768;
constexpr size_t WS_Z2F=WS_WOF+(size_t)90*36*1024;
constexpr size_t WS_Q=WS_Z2F+(size_t)16*32*FST1;
constexpr size_t WS_KV=WS_Q+(size_t)S*D*2;
constexpr size_t WS_ST=WS_KV+163840;
constexpr size_t WS_HF=WS_ST+(size_t)S*16*16;
constexpr size_t WS_END=WS_HF+(size_t)16*45*TST;
constexpr size_t WS_EPOCH=WS_FLAG+15*1024;
enum { FS=0, FW=1, FHH=2, FO=3, FQ=4, FL=8, FN=12, FG=13 };

struct In { const u16 *x,*wq,*wk,*wv,*wo,*w_in,*w_sp,*b_sp,*w_out; };

DI f32x2 gelu2(f32x2 x){
  const float A=-2.3022082f, Bc=-0.10294324f;
  f32x2 x2=x*x; f32x2 t=x2*Bc+A; f32x2 w=x*t;
  f32x2 e; e.x=__builtin_amdgcn_exp2f(w.x); e.y=__builtin_amdgcn_exp2f(w.y);
  e=e+1.0f; f32x2 r; r.x=__builtin_amdgcn_rcpf(e.x); r.y=__builtin_amdgcn_rcpf(e.y);
  return x*r; }
#define DPPADD(v,ctrl) v+=__builtin_bit_cast(float,__builtin_amdgcn_update_dpp(0,__builtin_bit_cast(int,v),ctrl,0xf,0xf,false))
DI void row16_sum8(const f32x2& a01,const f32x2& a23,const f32x2& b01,const f32x2& b23,float& ua,float& ub){ float t0,t1,t2,t3;
#define RD(d,x,c,m) "v_add_f32_dpp %" #d ", %" #x ", %" #x " " c " row_mask:0xf bank_mask:" #m "\n\t"
  asm volatile("s_nop 1\n\t"
    RD(0,6,"row_ror:8",0x3) RD(1,8,"row_ror:8",0x3) RD(2,10,"row_ror:8",0x3) RD(3,12,"row_ror:8",0x3)
    RD(0,7,"row_ror:8",0xc) RD(1,9,"row_ror:8",0xc) RD(2,11,"row_ror:8",0xc) RD(3,13,"row_ror:8",0xc)
    RD(4,0,"row_half_mirror",0x5) RD(4,1,"row_half_mirror",0xa) RD(5,2,"row_half_mirror",0x5) RD(5,3,"row_half_mirror",0xa)
    "s_nop 0\n\t" RD(4,4,"quad_perm:[1,0,3,2]",0xf) "s_nop 0\n\t" RD(5,5,"quad_perm:[1,0,3,2]",0xf) "s_nop 0\n\t" RD(4,4,"quad_perm:[2,3,0,1]",0xf) "s_nop 0\n\t" RD(5,5,"quad_perm:[2,3,0,1]",0xf)
    : "=&v"(t0),"=&v"(t1),"=&v"(t2),"=&v"(t3),"=&v"(ua),"=&v"(ub)
    : "v"(a01.x),"v"(a01.y),"v"(a23.x),"v"(a23.y),"v"(b01.x),"v"(b01.y),"v"(b23.x),"v"(b23.y)); }
DI float gsum(float v){ v+=__shfl_xor(v,16); v+=__shfl_xor(v,32); return v; }
DI void tile_map(int b,int& tb,int& fb){ const int xcd=b&7, idx=b>>3; tb=(xcd&3)*4+(idx&3); fb=(xcd>>2)*8+(idx>>2); }

DI void set_flag(char* ws,int id,int which,unsigned tag){ __hip_atomic_store((gu32*)(ws+WS_FLAG)+which*256+id,tag,RLX_AGENT); }
DI void poll_flags(char* ws,int id,int which,unsigned tag){
  gu32* p=(gu32*)(ws+WS_FLAG)+which*256+id;
  for(unsigned sp=0; sp<(1u<<16); sp++){ unsigned v=__hip_atomic_load(p,RLX_AGENT); if(__all(v==tag)) break; __builtin_amdgcn_s_sleep(4); } }
DI void wait_flags(char* ws,int id,int which,unsigned tag){ poll_flags(ws,id,which,tag); }
DI unsigned get_tag(char* ws){ return __builtin_amdgcn_readfirstlane(__hip_atomic_load((gu32*)(ws+WS_EPOCH),RLX_AGENT))+1u; }

struct Acc { f32x4 m[4][4]; f32x4 s[2]; };
struct Frg { u32x4 t[4], f[4], s; };
DI void acc_zero(Acc& a){
#pragma unroll
  for(int i=0;i<4;i++)
#pragma unroll
    for(int j=0;j<4;j++){ a.m[i][j]=(f32x4){0,0,0,0}; PIN(a.m[i][j]); }
  a.s[0]=(f32x4){0,0,0,0}; a.s[1]=(f32x4){0,0,0,0}; PIN(a.s[0]); PIN(a.s[1]); }
constexpr int G_F=32768, G_SC=G_F+4*9216;

#define ZPUB 1
DI void g_role(const In& in,char* ws,u16* out,char* lds,int id,int wid,int lane){
  int tb,fb; tile_map(id,tb,fb);
  const int wt=wid&1, wf=wid>>1; int r=lane&15, g=lane>>4, lr=lane>>3;
  const LAS char* L=(const LAS char*)lds;
  const unsigned lds0=(unsigned)(size_t)lds, vF=lane*16;
  unsigned vT[4], vW[3]; const char *gT, *gF;
  auto set_vT=[&](int pitch){
#pragma unroll
    for(int i=0;i<4;i++) vT[i]=(i*8+lr)*pitch+(((lane&7)^((lr>>1)+(i&1)*4))*16)-i*1024; };
  set_vT(1152);
#pragma unroll
  for(int i=0;i<3;i++){ const int c=(i<2? wid*2+i : 8)*64+lane, slot=c/18, ch=c-slot*18; vW[i]=swap23(slot)*(FF*2)+ch*16-(i==1? 1024:0); }
  const unsigned lT=lds0+wid*4096, lF=lds0+G_F+wid*2048, lF8=lds0+G_F+8192;
#define DT(q,i) do{ if((i)==0) glds(gT,vT[0],lT+((q)>>1)*16384); else gldo<(i)*1024>(gT,vT[i]); }while(0)
#define DW(q,i) do{ if((i)==0) glds(gF,vW[0],lF+(q)*9216); else gldo<1024>(gF,vW[1]); }while(0)
#define DW8(q) eglds<q>(wid,gF,vW[2],lF8+(q)*9216)
#define FW_(q) glds2(gF+wid*2048,vF,lF+(q)*9216)
#define FW8(q) eglds<q>(wid,gF+8192,vF,lF8+(q)*9216)
#define DF(q) do{ if(MODE<2){ DW(q,0); DW(q,1); } else FW_(q); }while(0)
#define DF8(q) do{ if(MODE<2) DW8(q); else FW8(q); }while(0)
#define T_POS(q) do{ DT(q,0); DT(q,1); DT(q,2); DT(q,3); gT+=128; }while(0)
#define F_POS(q) do{ DF(q); DF8(q); gF+= MODE<2? 32*FF*2:9216; }while(0)
  auto raw_start=[&](int col0){ constexpr int MODE=0; gT=(const char*)(in.x+(long)(tb*128+wid*32)*D); gF=(const char*)(in.w_in+col0);
    T_POS(2); F_POS(2); F_POS(3); T_POS(0); F_POS(0); F_POS(1); };
  const LAS char *tr_[2][2], *frm[2], *ffm[2];
  const int sl=(r>>2)+4*(g&1)+16*(g>>1);
#pragma unroll
  for(int p=0;p<2;p++){ const int u=wt*4+((2*p+2*wf)&3);
#pragma unroll
    for(int k=0;k<2;k++) tr_[k][p]=L+(u*16+r)*128+(((4*k+g)^((r>>1)&7))*16);
    const int v=wf*4+((2*p+2*wt)&3); frm[p]=L+G_F+sl*288+(lane&3)*8+v*32; }
  const LAS char* frs=L+G_F+sl*288+(lane&3)*8+256; const LAS char* ffs;
#define FU(fi) (wf*4+(((fi)+2*wt)&3))
  Acc acc; Frg fa,fb_;
#define RH(dst,h,ptr) do{ u32x2 v_=trd(ptr); dst[2*(h)]=v_[0]; dst[2*(h)+1]=v_[1]; }while(0)
#define RT(X,q,i) X.t[i]=LD(tr_[(q)&1][(i)>>1]+((q)>>1)*16384+((i)&1)*2048)
#define RF(X,q,j) do{ if(MODE<2){ RH(X.f[j],0,frm[(j)>>1]+(q)*9216+((j)&1)*32); RH(X.f[j],1,frm[(j)>>1]+(q)*9216+8*288+((j)&1)*32); } else X.f[j]=LD(ffm[(j)>>1]+(q)*9216+((j)&1)*1024); }while(0)
#define RS(X,q) do{ if(MODE<2){ RH(X.s,0,frs+(q)*9216); RH(X.s,1,frs+(q)*9216+8*288); } else X.s=LD(ffs+(q)*9216); }while(0)
#define RDALL(X,q) do{ _Pragma("unroll") for(int i=0;i<4;i++){ RT(X,q,i); RF(X,q,i); } RS(X,q); }while(0)
#define GG(MF,W) do{ MF; SBAR(); W; SBAR(); }while(0)
#define GM(fi,ti,X,XT) do{ if(MODE==0) MF16A(acc.m[fi][ti],XT[ti],X.f[fi]); else MF16A(acc.m[fi][ti],X.f[fi],XT[ti]); }while(0)
#define GS(ti,X,XT) do{ if(MODE==0) MF16A(acc.s[ti],XT[ti],X.s); else MF16A(acc.s[ti],X.s,XT[ti]); }while(0)
#define RT2(D,q,k,i) D[i]=LD(tr_[k][(i)>>1]+((q)>>1)*16384+((i)&1)*2048)
#define GSTEP(X,XT,Y,NT,q,DMA,VM) do{ constexpr int qn=((q)+1)&3; constexpr bool ET=(DMA)&&!((q)&1), OD=(q)&1; \
    SBAR(); WAIT_LGKM0(); GM(0,0,X,XT); GM(0,1,X,XT); WAIT_VM(VM); BARRIER(); SBAR(); \
    GG(GM(0,2,X,XT),if(ET) DT(q,0); else if(DMA) DF(q)); GG(GM(0,3,X,XT),if(ET) DT(q,1); else if(DMA) DF8(q)); GG(GM(1,0,X,XT),if(ET) DT(q,2); if(OD){ RT2(Y.t,qn,0,0); RF(Y,qn,0); }); \
    GG(GM(1,1,X,XT),if(ET) DT(q,3); if(OD){ RT2(Y.t,qn,0,1); RT2(Y.t,qn,0,2); }); GG(GM(1,2,X,XT),if(ET) DF(q); if(OD){ RT2(Y.t,qn,0,3); RF(Y,qn,1); }); GG(GM(1,3,X,XT),if(ET) DF8(q); if(OD) RF(Y,qn,2)); \
    GG(GM(2,0,X,XT),if(OD) RF(Y,qn,3); else RF(Y,qn,0)); GG(GM(2,1,X,XT),if(OD) RS(Y,qn); else RF(Y,qn,1)); GG(GM(2,2,X,XT),if(OD) RT2(NT,qn,1,0); else RF(Y,qn,2)); GG(GM(2,3,X,XT),if(OD) RT2(NT,qn,1,1); else RF(Y,qn,3)); \
    GG(GM(3,0,X,XT),if(OD) RT2(NT,qn,1,2); else RS(Y,qn)); GG(GM(3,1,X,XT),if(OD) RT2(NT,qn,1,3)); GM(3,2,X,XT); GM(3,3,X,XT); GS(0,X,XT); GS(1,X,XT); \
    if(DMA){ if(!((q)&1)) gT+=128; gF+= MODE<2? 32*FF*2:9216; } SBAR(); }while(0)
#define GQUAD(D,V0,V1,V2,V3) do{ GSTEP(fa,fa.t,fb_,tx,0,D,V0); GSTEP(fb_,fb_.t,fa,tx,1,D,V1); GSTEP(fa,fa.t,fb_,tx,2,D,V2); GSTEP(fb_,tx,fa,fb_.t,3,D,V3); }while(0)
#define SAMPLE(ix) gldw(ws+WS_FLAG,(unsigned)(ix)*4,lds0+G_SC+7424)
#define CHECK(ix) do{ if(!__all(*(const volatile LAS unsigned*)(L+G_SC+7424+lane*4)==tag)) poll_flags(ws,ix,0,tag); }while(0)
#define IX_LN (lane<16? FN*256+tb*16+lane : FL*256+(id&7)*128+(FH+fb*144+(lane&3)*143/3)/64)
#define IX_Z2 (lane<32? FG*256+tb*32+lane : FL*256+(id&7)*128+(fb*144+(lane&3)*143/3)/64)
  if(wid==0) gldw(ws+WS_EPOCH,0,lds0+G_SC+7168);
  raw_start(FH+fb*144);
  unsigned tag=0; u32x4 tx[4];
  acc_zero(acc);
  { constexpr int MODE=0;
    WAIT_VM(10); BARRIER(); RDALL(fa,2);
#pragma unroll
    for(int i=0;i<4;i++) RT2(tx,2,1,i);
    GSTEP(fa,fa.t,fb_,tx,2,true,8); GSTEP(fb_,tx,fa,fb_.t,3,true,8);
#pragma unroll 1
    for(int j=0;j<4;j++){
      if(j==2 && wid==0) SAMPLE(IX_LN);
      if(j==3){ gT-=9*128; gF=(const char*)(in.w_in+fb*144);
        if(wid==0){
          tag=__builtin_amdgcn_readfirstlane(*(const volatile LAS unsigned*)(L+G_SC+7168))+1u; CHECK(IX_LN);
          glds(ws+WS_LN+tb*1024,vF,lds0+G_SC+4096); glds(ws+WS_CS+(FH+fb*144)*4,vF,lds0+G_SC+5120); } }
      GQUAD(true,8,8,8,8); }
    MFMA_DRAIN(); SBAR(); }
    u32x2 z2m[4][4], z2s[2];
  { LAS char* stp=(LAS char*)lds+G_SC+wf*1024; float csl[5];
#pragma unroll
    for(int fi=0;fi<5;fi++) csl[fi]=*(const LAS float*)(L+G_SC+5120+((fi<4? FU(fi):8)*16+r)*4);
#pragma unroll
    for(int ti=0;ti<4;ti++){
      const int tu=wt*4+((ti+2*wf)&3);
      const f32x4 p0=*(const LAS f32x4*)(L+G_SC+4096+(tu*16+4*g)*8), p1=*(const LAS f32x4*)(L+G_SC+4096+(tu*16+4*g)*8+16);
      const f32x2 mu01={p0[0],p0[2]}, mu23={p1[0],p1[2]}, rs01={p0[1],p0[3]}, rs23={p1[1],p1[3]};
      f32x2 z01[5], z23[5];
#pragma unroll
      for(int fi=0;fi<5;fi++){ if(fi==4 && ti>=2) break;
        const f32x4 a= fi<4? acc.m[fi][ti] : acc.s[ti&1];
        f32x2 y0=gelu2(((f32x2){a[0],a[1]}-mu01*csl[fi])*rs01), y1=gelu2(((f32x2){a[2],a[3]}-mu23*csl[fi])*rs23);
        u32x2 p={cvt2(y0.x,y0.y),cvt2(y1.x,y1.y)}; PIN(p); if(fi<4) z2m[fi][ti]=p; else z2s[ti&1]=p;
        z01[fi]=(f32x2){bflo(p[0]),bfhi(p[0])}; z23[fi]=(f32x2){bflo(p[1]),bfhi(p[1])}; }
      const int nf= ti<2? 5:4; const float inv=1.0f/(16*nf);
      f32x2 sa=(z01[0]+z01[1])+(z01[2]+z01[3]), sb=(z23[0]+z23[1])+(z23[2]+z23[3]); if(ti<2){ sa+=z01[4]; sb+=z23[4]; }
      f32x2 qa=z01[0]*z01[0], qb=z23[0]*z23[0];
#pragma unroll
      for(int fi=1;fi<5;fi++) if(fi<nf){ qa+=z01[fi]*z01[fi]; qb+=z23[fi]*z23[fi]; }
      float su,sq; row16_sum8(sa,sb,qa,qb,su,sq);
      const float mn=su*inv;
      if(!(r&3)) *(LAS f32x2*)(stp+(tu*16+4*g+((r>>1)&2)+(r>>3))*8)=(f32x2){mn,__builtin_fmaxf(sq-su*mn,0.f)};
    }
    if(threadIdx.x==0) *(LAS unsigned*)((LAS char*)lds+G_SC+3072)=tag; }
  acc_zero(acc);
  WAIT_LGKM0(); BARRIER();
  tag=__builtin_amdgcn_readfirstlane(*(const LAS unsigned*)(L+G_SC+3072));
  if(wid<2){
    const int row=wid*64+lane; const f32x2 p0=*(const LAS f32x2*)(L+G_SC+row*8), p1=*(const LAS f32x2*)(L+G_SC+1024+row*8);
    const float n0= ((row>>4)&3)>>1? 64.f:80.f, n1=144.f-n0; const float m=(p0.x*n0+p1.x*n1)*(1.0f/144.f), d0=p0.x-m, d1=p1.x-m;
    st8(ws+WS_ST+(long)(tb*4+(row>>5))*4096+fb*256+(row&31)*8,(u32x2){__float_as_uint(m),__float_as_uint(p0.y+p1.y+n0*d0*d0+n1*d1*d1)}); }
  SBAR();
  { constexpr int MODE=1;
    RDALL(fa,0);
#pragma unroll
    for(int i=0;i<4;i++) RT2(fb_.t,0,1,i);
#pragma unroll 1
    for(int j=0;j<3;j++){ GQUAD(true,8,8,8,8);
      if(j==0 && wid<2 && lane==0) set_flag(ws,tb*32+wid*16+fb,FG,tag); }
    if(wid==0) SAMPLE(IX_Z2);
    GSTEP(fa,fa.t,fb_,tx,0,true,8); GSTEP(fb_,fb_.t,fa,tx,1,true,8);
    if(wid==0){ WAIT_VM(8); CHECK(IX_Z2); glds(ws+WS_CS+fb*576,vF,lds0+G_SC+6144); }
    GSTEP(fa,fa.t,fb_,tx,2,false,8);
    { const char* gs=ws+WS_ST+(long)(tb*4+wid)*4096; glds2(gs,vF,lT+16384); glds2(gs+2048,vF,lT+16384+2048); }
    GSTEP(fb_,tx,fa,fb_.t,3,false,6); GSTEP(fa,fa.t,fb_,tx,0,false,4); GSTEP(fb_,fb_.t,fa,tx,1,false,4);
    MFMA_DRAIN(); SBAR(); }
    PIN(lane); r=lane&15; g=lane>>4; lr=lane>>3;
  WAIT_VM0();
  { f32x2 gv[8]; float sm=0;
#pragma unroll
    for(int j=0;j<8;j++) gv[j]=*(const LAS f32x2*)(L+16384+wid*4096+(lane>>5)*2048+j*256+(lane&31)*8);
#pragma unroll
    for(int j=0;j<8;j++) sm+=gv[j].x;
    sm+=__shfl_xor(sm,32); const float mu=sm*(1.0f/16); float M2=0;
#pragma unroll
    for(int j=0;j<8;j++){ const float d=gv[j].x-mu; M2+=gv[j].y+144.f*d*d; }
    M2+=__shfl_xor(M2,32); const float rs=1.0f/sqrtf(M2*(1.0f/FH)+1e-5f);
    if(lane<32) *(LAS f32x2*)((LAS char*)lds+G_SC+2048+(wid*32+lane)*8)=(f32x2){rs,-mu*rs}; }
  WAIT_LGKM0(); BARRIER();
  { char* z2f=ws+WS_Z2F+(long)fb*32*FST1; u32x2 ps[2];
#pragma unroll
    for(int ti=0;ti<4;ti++){
      const int tu=wt*4+((ti+2*wf)&3); const int s0=tb*128+tu*16;
      char* zb=z2f+(long)(s0>>5)*9216+((((s0>>4)&1)*2+(g>>1))*16+r)*16;
      const f32x4 p0=*(const LAS f32x4*)(L+G_SC+2048+(tu*16+4*g)*8), p1=*(const LAS f32x4*)(L+G_SC+2048+(tu*16+4*g)*8+16);
      const f32x2 rs01={p0[0],p0[2]}, rs23={p1[0],p1[2]}, nb01={p0[1],p0[3]}, nb23={p1[1],p1[3]};
      auto nz=[&](u32x2 a){ const f32x2 y0=(f32x2){bflo(a[0]),bfhi(a[0])}*rs01+nb01, y1=(f32x2){bflo(a[1]),bfhi(a[1])}*rs23+nb23; return (u32x2){cvt2(y0.x,y0.y),cvt2(y1.x,y1.y)}; };
      st16(zb+(FU(0)+(g&1))*1024,pair16(nz(z2m[0][ti]),nz(z2m[1][ti]))); st16(zb+(FU(2)+(g&1))*1024,pair16(nz(z2m[2][ti]),nz(z2m[3][ti])));
      if(ti<2) ps[ti]=nz(z2s[ti]);
      if(ti==1){ const int sA=tb*128+(wt*4+2*wf)*16;
        st16(z2f+(long)(sA>>5)*9216+8*1024+(((g&1)*2+(g>>1))*16+r)*16,pair16(ps[0],ps[1])); } } }
    SBAR();
  unsigned z1m[4][4][2], z1s[2][2]; float bb[4]; f32x2 rst[4];
#pragma unroll
  for(int ti=0;ti<4;ti++) rst[ti]=*(const LAS f32x2*)(L+G_SC+4096+((wt*4+((ti+2*wf)&3))*16+r)*8);
#pragma unroll
  for(int fi=0;fi<5;fi++){ const f32x4 c4=*(const LAS f32x4*)(L+G_SC+6144+((fi<4? FU(fi):8)*16+4*g)*4);
#pragma unroll
    for(int ti=0;ti<4;ti++){ if(fi==4 && ti>=2) break;
      const f32x4 a= fi<4? acc.m[fi][ti] : acc.s[ti&1];
      f32x2 y0=gelu2(((f32x2){a[0],a[1]}-(f32x2){c4[0],c4[1]}*rst[ti].x)*rst[ti].y), y1=gelu2(((f32x2){a[2],a[3]}-(f32x2){c4[2],c4[3]}*rst[ti].x)*rst[ti].y);
      unsigned w0=cvt2(y0.x,y0.y), w1=cvt2(y1.x,y1.y); PIN(w0); PIN(w1);
      if(fi<4){ z1m[fi][ti][0]=w0; z1m[fi][ti][1]=w1; } else { z1s[ti&1][0]=w0; z1s[ti&1][1]=w1; } }
    if(fi==ZPUB){
      SBAR(); WAIT_VM0(); BARRIER(); if(threadIdx.x==0) set_flag(ws,fb*16+tb,FS,tag);       constexpr int MODE=2;
      set_vT(4096);
      gT=(const char*)(in.w_sp+(long)(tb*128+wid*32)*S);
      T_POS(0); T_POS(2); SBAR(); } }
  acc_zero(acc);
  u16 bsv[4];
#pragma unroll
  for(int ti=0;ti<4;ti++) bsv[ti]=in.b_sp[tb*128+(wt*4+((ti+2*wf)&3))*16+r];
    if(wid==0) poll_flags(ws,fb*16+(lane&15),FS,tag);
  BARRIER();
  { int l16=lane*16; PIN(l16); ffm[0]=L+G_F+(wf*4+((2*wt)&3))*1024+l16; ffm[1]=L+G_F+(wf*4+((2+2*wt)&3))*1024+l16; ffs=L+G_F+8192+l16; }
  gF=ws+WS_Z2F+(long)fb*32*FST1;
  { constexpr int MODE=2; F_POS(0); F_POS(1); F_POS(2); F_POS(3); }
  { constexpr int MODE=2;
    WAIT_VM(6); BARRIER(); RDALL(fa,0);
#pragma unroll
    for(int i=0;i<4;i++) RT2(fb_.t,0,1,i);
    GQUAD(true,4,8,8,8);
#pragma unroll 1
    for(int j=0;j<14;j++) GQUAD(true,8,8,8,8);
    GQUAD(false,8,2,0,0); MFMA_DRAIN(); SBAR(); }
  #pragma unroll
  for(int ti=0;ti<4;ti++) bb[ti]=bflo(bsv[ti]);
  { char* hf=ws+WS_HF+(long)tb*45*TST;
    auto hv=[&](const f32x4& a,unsigned z0,unsigned z1,float bv){
      return (u32x2){cvt2(bflo(z0)*(a[0]+bv),bfhi(z0)*(a[1]+bv)),cvt2(bflo(z1)*(a[2]+bv),bfhi(z1)*(a[3]+bv))}; };
    auto cell=[&](int tu,int f16){ const int f=f16+(g>>1)*8; return hf+(long)(f>>5)*8192+tu*1024+(((f>>3)&3)*16+r)*16; };
#pragma unroll
    for(int ti=0;ti<4;ti++){ const int tu=wt*4+((ti+2*wf)&3);
#pragma unroll
      for(int fi=0;fi<4;fi+=2)
        st16(cell(tu,fb*144+(FU(fi)+(g&1))*16),pair16(hv(acc.m[fi][ti],z1m[fi][ti][0],z1m[fi][ti][1],bb[ti]),hv(acc.m[fi+1][ti],z1m[fi+1][ti][0],z1m[fi+1][ti][1],bb[ti]))); }
    st16(cell(wt*4+2*wf+(g&1),fb*144+128),pair16(hv(acc.s[0],z1s[0][0],z1s[0][1],bb[0]),hv(acc.s[1],z1s[1][0],z1s[1][1],bb[1]))); }
  WAIT_VM0(); WAIT_LGKM0(); BARRIER();
  if(threadIdx.x==0) set_flag(ws,tb*16+fb,FHH,tag);
  { const int th=fb&1, jc=fb>>1, nf=4+(jc&1), du0=(jc>>1)*9+(jc&1)*4, kk=wid>>1;
    if(wid==0) wait_flags(ws,tb*16+(lane&15),FHH,tag);
    else { poll_flags(ws,wid*64+lane,FW,tag); if(wid==1) poll_flags(ws,lane,FW,tag); }
    WAIT_VM0(); BARRIER();
    const char* gT4=ws+WS_HF+(long)tb*45*TST+(kk*8+th*4+wt*2)*1024; const char* gF4=ws+WS_WOF+((long)kk*36+du0+wt*2)*1024;
    const unsigned lT4=lds0+(kk*4+wt*2)*1024, lF4=lds0+8192+kk*5120+wt*2048; const int x5=(jc&1)+2*wt;
#define D4(q) do{ glds2(gT4,vF,lT4+(q)*18432); glds2(gF4,vF,lF4+(q)*18432); eglds<1>(x5,gF4+4096,vF,lF4+4096+(q)*18432); gT4+=TST; gF4+=72*1024; }while(0)
    f32x4 a4[5][2];
#pragma unroll
    for(int n=0;n<5;n++){ a4[n][0]=(f32x4){0,0,0,0}; a4[n][1]=(f32x4){0,0,0,0}; PIN(a4[n][0]); PIN(a4[n][1]); }
    const LAS char* t4=L+(kk*4+wt*2)*1024+lane*16; const LAS char* f4=L+8192+kk*5120+lane*16;
    struct F4 { u32x4 t0,t1,f[5]; } xa,xb;
#define R4(X,q) do{ X.t0=LD(t4+(q)*18432); X.f[0]=LD(f4+(q)*18432); X.t1=LD(t4+(q)*18432+1024); _Pragma("unroll") for(int n=1;n<5;n++) X.f[n]=LD(f4+(q)*18432+n*1024); }while(0)
#define M4(n,X) do{ MF16A(a4[n][0],X.f[n],X.t0); MF16A(a4[n][1],X.f[n],X.t1); SBAR(); }while(0)
#define S4(X,Y,q,DMA,VM) do{ constexpr int o_=(((q)+1)&3)*18432; SBAR(); WAIT_LGKM0(); M4(0,X); WAIT_VM(VM); BARRIER(); SBAR(); \
      Y.t0=LD(t4+o_); Y.f[0]=LD(f4+o_); Y.t1=LD(t4+o_+1024); Y.f[1]=LD(f4+o_+1024); SBAR(); if(DMA) glds(gT4,vF,lT4+(q)*18432); SBAR(); M4(1,X); \
      Y.f[2]=LD(f4+o_+2048); Y.f[3]=LD(f4+o_+3072); Y.f[4]=LD(f4+o_+4096); SBAR(); if(DMA) gldo<1024>(gT4,vF); SBAR(); M4(2,X); \
      if(DMA) glds(gF4,vF,lF4+(q)*18432); SBAR(); M4(3,X); if(DMA) gldo<1024>(gF4,vF); SBAR(); M4(4,X); \
      if(DMA){ eglds<1>(x5,gF4+4096,vF,lF4+4096+(q)*18432); gT4+=TST; gF4+=72*1024; } SBAR(); }while(0)
    D4(0); D4(1); D4(2); D4(3);
    WAIT_VM(12); BARRIER(); R4(xa,0);
#pragma unroll 1
    for(int j=0;j<10;j++){
      if(j==7 && wid==0) SAMPLE(FO*256+tb*16+th*8+(lane&7));
      S4(xa,xb,0,true,8); S4(xb,xa,1,true,8); S4(xa,xb,2,true,8);
      if(j==7){ if(wid==0) CHECK(FO*256+tb*16+th*8+(lane&7)); }
      S4(xb,xa,3,true,8); }
    S4(xa,xb,0,true,8);
    const long row=(long)(tb*128+th*64+(wt*2+kk)*16+r)*D+du0*16+4*g; u32x2 xv[5];
#pragma unroll
    for(int n=0;n<5;n++) xv[n]=ld8u(in.x+row+(n<nf? n:0)*16);
    SBAR();
    S4(xb,xa,1,false,13); S4(xa,xb,2,false,9); S4(xb,xa,3,false,5); S4(xa,xb,0,false,5);
    MFMA_DRAIN(); WAIT_LGKM0(); BARRIER();
    f32x4* ex=(f32x4*)lds+lane;
#pragma unroll
    for(int n=0;n<5;n++) ex[(wid*5+n)*64]= kk? a4[n][0]:a4[n][1];
    WAIT_LGKM0(); BARRIER();
#pragma unroll
    for(int n=0;n<5;n++) if(n<nf){ const f32x4 a=(kk? a4[n][1]:a4[n][0])+ex[((wid^2)*5+n)*64];
      *(u32x2*)(out+row+n*16)=(u32x2){cvt2(bflo(xv[n][0])+a[0],bfhi(xv[n][0])+a[1]),cvt2(bflo(xv[n][1])+a[2],bfhi(xv[n][1])+a[3])}; }
      }
}

#define QSLEEP 4
#define PRIO_G 1
#define PRIO_A 2
#define PRIO_P 0
#define PRIO_T 0
DI float max3f(float a,float b,float c){ return __builtin_fmaxf(__builtin_fmaxf(a,b),c); }
DI float tmax16(const f32x16& s){
  float a=max3f(s[0],s[1],s[2]), b=max3f(s[3],s[4],s[5]); a=max3f(a,s[6],s[7]); b=max3f(b,s[8],s[9]); a=max3f(a,s[10],s[11]); b=max3f(b,s[12],s[13]); a=max3f(a,s[14],s[15]); return __builtin_fmaxf(a,b); }
DI float xhalf_max(float m){ auto rr=__builtin_amdgcn_permlane32_swap(__float_as_uint(m),__float_as_uint(m),false,false); return __builtin_fmaxf(__uint_as_float(rr[0]),__uint_as_float(rr[1])); }
#define EXP_NQ 8
#define ATT_BIAS 80.f
#define ATT_THR 1.2676506e30f
DI float set_m(unsigned& w,unsigned qw,float m,bool low){
  unsigned hi=cvt2(-m,0.f)&0xffffu; float fh=__uint_as_float(hi<<16); float rem=-m-fh; unsigned lo=cvt2(rem,0.f)&0xffffu; float fl=__uint_as_float(lo<<16);
  w= low? qw : (hi|(lo<<16)); return -(fh+fl); }
DI void transpose8x8(const u32x4 (&in)[8],u32x4 (&out)[8]){
#pragma unroll
  for(int n2=0;n2<4;n2++)
#pragma unroll
    for(int k2=0;k2<4;k2++){ unsigned a=in[2*k2][n2], b=in[2*k2+1][n2];
      out[2*n2][k2]=__builtin_amdgcn_perm(b,a,0x05040100u); out[2*n2+1][k2]=__builtin_amdgcn_perm(b,a,0x07060302u); } }

template<int KIND>
DI void a_proj(const In& in,char* ws,char* lds,int tb,int fb,int wid,int lane,unsigned& tag){
  const int r=lane&15, g=lane>>4, lr=lane>>3;
  const LAS char* L=(const LAS char*)lds; const unsigned lds0=(unsigned)(size_t)lds;
  constexpr int NT=3, FO_=NT*16384, NU= KIND==0? 3:2, XP= KIND==0? 1152:36;
  unsigned vt[4], vf[2];
#pragma unroll
  for(int i=0;i<4;i++){ const int ii=wid*4+i; vt[i]=(8*ii+lr)*1152+(((lane&7)^(((lr>>1)+4*ii)&7))*16); }
  const int fp0= wid<2? wid*2 : 2+wid, nfp= wid<2? 2:1;
#pragma unroll
  for(int i=0;i<2;i++){ int c=(fp0+(i<nfp? i:0))*64+lane; int slot=c/6, ch=c-slot*6; vf[i]=swap23(slot)*XP+(KIND==0 || ch<2? ch*16:20); }
  const char* gt=(const char*)(in.x+(long)tb*128*D); const char* gf=(const char*)(KIND==0? in.wq+fb*48 : KIND==1? in.wk:in.wv);
  auto stage=[&](int s){ const unsigned mt=lds0+(s%NT)*16384+wid*4096;
#pragma unroll
    for(int i=0;i<4;i++) glds(gt+s*128,vt[i],mt+i*1024);
    const unsigned mf=lds0+FO_+(s%NT)*6144+fp0*1024; glds(gf+(long)s*64*XP,vf[0],mf); cglds<1>(nfp,gf+(long)s*64*XP,vf[1],mf+1024); };
#pragma unroll
  for(int s=0;s<NT-1;s++) stage(s);
  f32x4 acc[3][2], c4[3]; const u32x4 ones={ONES,ONES,ONES,ONES};
#pragma unroll
  for(int n=0;n<3;n++){ acc[n][0]=(f32x4){0,0,0,0}; acc[n][1]=(f32x4){0,0,0,0}; c4[n]=(f32x4){0,0,0,0}; }
  const int sl=(r>>2)+4*(g&1)+16*(g>>1);
#pragma unroll 1
  for(int s=0;s<9;s++){
    WAIT_LGKM0();
    if(NT==3 && s<8){ if(wid<2) asm volatile("s_waitcnt vmcnt(6)":::"memory"); else asm volatile("s_waitcnt vmcnt(5)":::"memory"); } else WAIT_VM0();
    BARRIER();
    if(s+NT-1<9) stage(s+NT-1);
    const LAS char* T=L+(s%NT)*16384+(wid*32+r)*128;
#pragma unroll
    for(int k=0;k<2;k++){
      const int ch=((4*k+g)^((r>>1)&7))*16;
      u32x4 t0=LD(T+ch), t1=LD(T+2048+ch), f[3];
#pragma unroll
      for(int n=0;n<NU;n++){
        const LAS char* p=L+FO_+(s%NT)*6144+(sl+32*k)*96+(lane&3)*8+n*32; u32x2 lo=trd(p), hi=trd(p+8*96); f[n]=(u32x4){lo[0],lo[1],hi[0],hi[1]}; }
#pragma unroll
      for(int n=0;n<NU;n++){
        if(KIND==2){ acc[n][0]=MF16(t0,f[n],acc[n][0]); acc[n][1]=MF16(t1,f[n],acc[n][1]); c4[n]=MF16(ones,f[n],c4[n]); }
        else { acc[n][0]=MF16(f[n],t0,acc[n][0]); acc[n][1]=MF16(f[n],t1,acc[n][1]); c4[n]=MF16(f[n],ones,c4[n]); } } }
  }
  if(wid==0){ tag=get_tag(ws); poll_flags(ws,tb*16+(lane&15),FN,tag); }
  BARRIER();
  const char* lnb=ws+WS_LN+(long)(tb*128+wid*32)*8;
  char* kv=ws+WS_KV+(long)tb*KVCH;
  if(KIND<2){ f32x2 st[2];
    st[0]=ld8(lnb+r*8); st[1]=ld8(lnb+(16+r)*8);
    if(KIND==0){ char* q=ws+WS_Q+((long)(tb*128+wid*32+(g&1)*16+r)*D+fb*48+(g>>1)*8)*2;
#pragma unroll
      for(int n=0;n<3;n++){ const f32x4 a=(acc[n][0]-c4[n]*st[0].x)*(st[0].y*QSCALE), b=(acc[n][1]-c4[n]*st[1].x)*(st[1].y*QSCALE);
        st16(q+n*32,pair16((u32x2){cvt2(a[0],a[1]),cvt2(a[2],a[3])},(u32x2){cvt2(b[0],b[1]),cvt2(b[2],b[3])})); } }
    else {
      const f32x4 a=(acc[0][0]-c4[0]*st[0].x)*st[0].y, b=(acc[0][1]-c4[0]*st[1].x)*st[1].y, c=(acc[1][0]-c4[1]*st[0].x)*st[0].y, d=(acc[1][1]-c4[1]*st[1].x)*st[1].y;
      st16(kv+KV_K0+wid*1024+(g*16+r)*16,pair16((u32x2){cvt2(a[0],a[1]),cvt2(a[2],a[3])},(u32x2){cvt2(b[0],b[1]),cvt2(b[2],b[3])}));
      if(g==1 || lane==32) st4(kv+KV_K1+wid*128+(g==1? r*4:512),g==1? cvt2(c[2],c[3]):ONES);
      if(g==1) st4(kv+KV_K1+wid*128+64+r*4,cvt2(d[2],d[3])); } }
  else { const float c0=c4[0][0], c1=c4[1][0]; u32x2 pa[2], pc[2];
#pragma unroll
    for(int ti=0;ti<2;ti++){ f32x4 mu,rs;
#pragma unroll
      for(int e=0;e<4;e++){ f32x2 p=ld8(lnb+(ti*16+4*g+e)*8); mu[e]=p.x; rs[e]=p.y; }
      const f32x4 a=(acc[0][ti]-mu*c0)*rs, c=(acc[1][ti]-mu*c1)*rs;
      pa[ti]=(u32x2){cvt2(a[0],a[1]),cvt2(a[2],a[3])}; pc[ti]=(u32x2){cvt2(c[0],c[1]),cvt2(c[2],c[3])}; }
    char* vb=kv+KV_V+wid*VT+g*VH; u32x4 x=pair32(pc[0],pc[1]); if(r==8) x=ones; if(r==9) x=(u32x4){0,0,0,0};
    st16(vb+r*16,pair32(pa[0],pa[1]));
    if(r>=6 && r<=(VH==320? 9:8)) st16(vb+(10+r)*16,x); }
}

DI void a_rowstat(const In& in,char* ws,int tb,int fb,int lane){
  const int lr=lane>>3, c8=lane&7, row=tb*128+fb*8+lr; u32x4 v[9];
  const unsigned ep=__hip_atomic_load((gu32*)(ws+WS_EPOCH),RLX_AGENT);
#pragma unroll
  for(int s=0;s<9;s++) v[s]=*(const u32x4*)(in.x+(long)row*D+c8*8+s*64);
  f32x2 sm={0,0};
#pragma unroll
  for(int s=0;s<9;s++)
#pragma unroll
    for(int e=0;e<4;e++) sm+=(f32x2){bflo(v[s][e]),bfhi(v[s][e])};
  float t=sm.x+sm.y; DPPADD(t,0xB1); DPPADD(t,0x4E); DPPADD(t,0x141); const float mu=t*(1.0f/D);
  f32x2 q={0,0};
#pragma unroll
  for(int s=0;s<9;s++)
#pragma unroll
    for(int e=0;e<4;e++){ f32x2 d=(f32x2){bflo(v[s][e]),bfhi(v[s][e])}-mu; q+=d*d; }
  t=q.x+q.y; DPPADD(t,0xB1); DPPADD(t,0x4E); DPPADD(t,0x141);
  if(c8==0) st8(ws+WS_LN+(long)row*8,(u32x2){__float_as_uint(mu),__float_as_uint(1.0f/sqrtf(t*(1.0f/D)+1e-5f))});
  WAIT_VM0();
  if(lane==0) set_flag(ws,tb*16+fb,FN,ep+1u);
}

DI void a_colsum(const In& in,char* ws,char* lds,int c,int wid,int lane){
  const int tid=wid*64+lane; float* red=(float*)lds;
  const int c8=tid&7, rg=tid>>3;
  const u16* p=in.w_in+c*64+(long)rg*FF+c8*8; u32x4 v[18];
#pragma unroll
  for(int i=0;i<18;i++) v[i]=*(const u32x4*)(p+(long)i*32*FF);
  f32x2 a[4]={{0,0},{0,0},{0,0},{0,0}};
#pragma unroll
  for(int i=0;i<18;i++)
#pragma unroll
    for(int e=0;e<4;e++) a[e]+=(f32x2){bflo(v[i][e]),bfhi(v[i][e])};
#pragma unroll
  for(int e=0;e<4;e++) *(f32x2*)(red+rg*64+c8*8+2*e)=a[e];
  WAIT_LGKM0(); BARRIER();
  if(tid<64){ float s=0;
#pragma unroll
    for(int i=0;i<32;i++) s+=red[i*64+tid];
    st4(ws+WS_CS+(c*64+tid)*4,__float_as_uint(s)); }
}

DI void a_role(const In& in,char* ws,char* lds,int id,int aw,int lane){
  int tb,fb; tile_map(id,tb,fb);
    unsigned tagv=0, tag=0; if(aw==0) tagv=__hip_atomic_load((gu32*)(ws+WS_EPOCH),RLX_AGENT);
  { const int c0=((fb+2)&15)*16+tb, c= c0<36? c0+36:c0-36;
    if(c0<72){ a_colsum(in,ws,lds,c,aw,lane);
      tag=__builtin_amdgcn_readfirstlane(tagv)+1u;
      WAIT_VM0(); if(threadIdx.x<8) set_flag(ws,threadIdx.x*128+c,FL,tag);
      WAIT_LGKM0(); BARRIER(); }
    else if(fb>=3 && (aw==1 || (aw==2 && fb<8))) a_rowstat(in,ws,tb,fb-3+(aw-1)*11,lane); }
  auto relayout=[&](){
  const int g=lane>>4;
#pragma unroll 1
  for(int task= fb>=14? ((fb-14)*16+tb)*4+aw : fb>=12 && aw? 384+((fb-12)*16+tb)*3+aw-1 : 450; task<(fb>=14? 384:450); task+=128){
    if(task<450){ const int k32=task/5, cb=task-k32*5, c=cb*128+(lane&15)*8;
      const u16* src= k32<72? in.w_out+(long)k32*32*D : in.wo+(long)(k32-72)*32*D;
      u32x4 rin[8], rout[8]; const int cc= c<D? c:0;
#pragma unroll
      for(int j=0;j<8;j++) rin[j]=*(const u32x4*)(src+(long)(g*8+j)*D+cc);
      transpose8x8(rin,rout);
      LAS char* bw=(LAS char*)lds+aw*8192;
#pragma unroll
      for(int n=0;n<8;n++) *(LAS u32x4*)(bw+((lane&15)>>1)*1024+g*256+((lane&1)*8+n)*16)=rout[n];
      WAIT_LGKM0(); asm volatile("":::"memory");
      char* dst=ws+WS_WOF+((long)k32*36+cb*8)*1024+lane*16; const int nu= cb<4? 8:4;
#pragma unroll
      for(int u=0;u<8;u++){ u32x4 v=LD(bw+u*1024+lane*16); if(u<nu) st16(dst+u*1024,v); }
      WAIT_LGKM0(); } }
  };
#define RELAYOUT() relayout()
    __builtin_amdgcn_s_setprio(PRIO_P);
  if(fb<12) a_proj<0>(in,ws,lds,tb,fb,aw,lane,tag); else if(fb==12) a_proj<1>(in,ws,lds,tb,fb,aw,lane,tag); else if(fb==13) a_proj<2>(in,ws,lds,tb,fb,aw,lane,tag);
  WAIT_VM0(); WAIT_LGKM0(); BARRIER();
  if(fb<12){ if(threadIdx.x==0) set_flag(ws,tb*64+32+fb,FQ,tag); } else if(fb<14 && threadIdx.x<16) set_flag(ws,threadIdx.x*64+(fb-12)*16+tb,FQ,tag);
    RELAYOUT();
  __builtin_amdgcn_s_setprio(PRIO_A);
  if(aw==0){ gu32* p=(gu32*)(ws+WS_FLAG)+FQ*256+tb*64+(lane<44? lane:0);
    for(unsigned sp=0; sp<(1u<<16); sp++){ unsigned v=__hip_atomic_load(p,RLX_AGENT); if(__all(v==tag)) break; __builtin_amdgcn_s_sleep(QSLEEP); } }
  WAIT_VM0(); BARRIER();
  if(threadIdx.x==0) set_flag(ws,id,FW,tag);
    const int r=lane&31, hh=lane>>5; const bool low=(hh==0);
  const LAS char* L=(const LAS char*)lds; const unsigned lb=(unsigned)(size_t)lds, vF=lane*16;
  auto stageKV=[&](int q4){ const char* gsrc=ws+WS_KV+(long)q4*2*KVCH; const unsigned m=lb+(q4&3)*2*KVCH;
#pragma unroll
    for(int i=0;i<5;i++){ int off=(aw*5+i)*1024; if(off>2*KVCH-1024) off=2*KVCH-1024; glds(gsrc+off,vF,m+off); } };
  const int sa=tb*128+fb*8+aw*2;
  u32x4 qa0,qb0; unsigned qaw,qbw;
  { const char* pa=ws+WS_Q+(long)sa*1152+r*36; const char* pb=pa+1152;
    const char* ph=pa+hh*16; (void)pb;
    stageKV(0);
    asm volatile("global_load_dwordx4 %0, %5, off sc1\n\tglobal_load_dwordx4 %1, %5, off offset:1152 sc1\n\tglobal_load_dword %2, %4, off offset:32 sc1\n\tglobal_load_dword %3, %4, off offset:1184 sc1\n\ts_waitcnt vmcnt(0)"
                 : "=&v"(qa0),"=&v"(qb0),"=&v"(qaw),"=&v"(qbw) : "v"(pa),"v"(ph) : "memory"); }
  BARRIER();
  stageKV(1); stageKV(2); stageKV(3);
  const int k0off=KV_K0+lane*16, k1off=KV_K1+(low? r*4:512), voff=KV_V+hh*VH+(VH==320 && r>19? 19:r)*16;
  const f32x16 Z={0,0,0,0,0,0,0,0,0,0,0,0,0,0,0,0};
  unsigned wa= low? qaw:0u, wb= low? qbw:0u;
  float ma=0.f, mb=0.f;
  f32x16 oa0=Z,oa1=Z,ob0=Z,ob1=Z;
  u32x4 k1,ka,kb,va0,va1,vb0,vb1;
#define LDK1(B) do{ _Pragma("unroll") for(int t_=0;t_<4;t_++) k1[t_]=*(const LAS unsigned*)(B+k1off+t_*128); }while(0)
#define K1P(t) ((u32x2){k1[(t)&2],k1[((t)&2)+1]})
#define QE(w,t) (((t)&1)? (u32x2){0u,w} : (u32x2){w,0u})
#define QK1(S_,w,t) S_=MF32K8(K1P(t),QE(w,t),S_)
#define CHUNKMAX(B,xa,xb) do{ LDK1(B); _Pragma("unroll") for(int t=0;t<4;t++){ ka=LD(B+k0off+t*1024); \
      f32x16 s_=MF32(ka,qa0,Z); QK1(s_,wa,t); xa=__builtin_fmaxf(xa,tmax16(s_)); \
      f32x16 u_=MF32(ka,qb0,Z); QK1(u_,wb,t); xb=__builtin_fmaxf(xb,tmax16(u_)); } xa=xhalf_max(xa); xb=xhalf_max(xb); }while(0)
  { float xa=-3.0e38f, xb=-3.0e38f; CHUNKMAX(L,xa,xb); ma=set_m(wa,qaw,xa+ATT_BIAS,low); mb=set_m(wb,qbw,xb+ATT_BIAS,low); }
    __builtin_amdgcn_s_setprio(PRIO_T);
#define EXP4(S_,i) do{ S_[i]=__builtin_amdgcn_exp2f(S_[i]); S_[i+1]=__builtin_amdgcn_exp2f(S_[i+1]); S_[i+2]=__builtin_amdgcn_exp2f(S_[i+2]); S_[i+3]=__builtin_amdgcn_exp2f(S_[i+3]); }while(0)
#define GRP(MF,S_,i,P,j) do{ MF; SBAR(); EXP4(S_,i); unsigned w0_=cvt2(S_[i],S_[i+1]), w1_=cvt2(S_[i+2],S_[i+3]); asm volatile("":"+v"(w0_),"+v"(w1_)); P[j]=w0_; P[j+1]=w1_; SBAR(); }while(0)
#define CHUNK(B,IA,IB,OA,OB) do{ \
    f32x16 sa_,sb_; u32x4 pa0,pa1,pb0,pb1; \
    ka=LD(B+k0off); LDK1(B); kb=LD(B+k0off+1024); va0=LD(B+voff); va1=LD(B+voff+2*VH); \
    sa_=MF32(ka,qa0,Z); QK1(sa_,wa,0); SBAR(); \
    _Pragma("unroll") for(int t=0;t<4;t+=2){ \
      GRP(sb_=MF32(ka,qb0,Z),sa_,0,pa0,0); \
      GRP(QK1(sb_,wb,t),sa_,4,pa0,2); \
      if(t>0){ GRP(OB=MF32(vb0,pb0,OB),sa_,8,pa1,0); GRP(OB=MF32(vb1,pb1,OB),sa_,12,pa1,2); } \
      else   { GRP((void)0,sa_,8,pa1,0); GRP((void)0,sa_,12,pa1,2); } \
      vb0=LD(B+voff+(t+1)*VT); vb1=LD(B+voff+(t+1)*VT+2*VH); if(t+2<4) ka=LD(B+k0off+(t+2)*1024); SBAR(); \
      GRP(sa_=MF32(kb,qa0,Z),sb_,0,pb0,0); \
      GRP(QK1(sa_,wa,t+1),sb_,4,pb0,2); \
      if(t==0){ GRP(OA=MF32(va0,pa0,IA),sb_,8,pb1,0); } else { GRP(OA=MF32(va0,pa0,OA),sb_,8,pb1,0); } \
      GRP(OA=MF32(va1,pa1,OA),sb_,12,pb1,2); \
      GRP(sb_=MF32(kb,qb0,Z),sa_,0,pa0,0); \
      GRP(QK1(sb_,wb,t+1),sa_,4,pa0,2); \
      if(t==0){ GRP(OB=MF32(va0,pb0,IB),sa_,8,pa1,0); } else { GRP(OB=MF32(va0,pb0,OB),sa_,8,pa1,0); } \
      GRP(OB=MF32(va1,pb1,OB),sa_,12,pa1,2); \
      if(t+2<4){ va0=LD(B+voff+(t+2)*VT); va1=LD(B+voff+(t+2)*VT+2*VH); kb=LD(B+k0off+(t+3)*1024); } SBAR(); \
      if(t+2<4){ GRP(sa_=MF32(ka,qa0,Z),sb_,0,pb0,0); GRP(QK1(sa_,wa,t+2),sb_,4,pb0,2); } \
      else     { GRP((void)0,sb_,0,pb0,0); GRP((void)0,sb_,4,pb0,2); } \
      GRP(OA=MF32(vb0,pa0,OA),sb_,8,pb1,0); \
      GRP(OA=MF32(vb1,pa1,OA),sb_,12,pb1,2); \
    } \
    OB=MF32(vb0,pb0,OB); OB=MF32(vb1,pb1,OB); \
  }while(0)
#define RESCALE(B,IA,IB) do{ float xa=-ATT_BIAS, xb=-ATT_BIAS; CHUNKMAX(B,xa,xb); \
    float na=set_m(wa,qaw,ma+xa+ATT_BIAS,low), nb=set_m(wb,qbw,mb+xb+ATT_BIAS,low); float fa_=__builtin_amdgcn_exp2f(0.5f*(ma-na)), fb_=__builtin_amdgcn_exp2f(0.5f*(mb-nb)); ma=na; mb=nb;   \
    _Pragma("unroll") for(int i=0;i<16;i++){ IA[i]=(IA[i]*fa_)*fa_; IB[i]=(IB[i]*fb_)*fb_; } }while(0)
#define BAD(OA,OB) (__any( low && !((OA[10]<=ATT_THR) && (OB[10]<=ATT_THR)) ))
#pragma unroll 1
  for(int q4=0;q4<EXP_NQ;q4++){
    { const LAS char* B=L+(q4&3)*2*KVCH;
      for(int att=0;;att++){ CHUNK(B,oa0,ob0,oa1,ob1);
        if(__builtin_expect(att>0 || !BAD(oa1,ob1),1)) break; RESCALE(B,oa0,ob0); } }
    { const LAS char* B=L+(q4&3)*2*KVCH+KVCH;
      for(int att=0;;att++){ CHUNK(B,oa1,ob1,oa0,ob0);
        if(__builtin_expect(att>0 || !BAD(oa0,ob0),1)) break; RESCALE(B,oa1,ob1); } }
    if(q4<5){
      WAIT_VM0(); WAIT_LGKM0(); BARRIER();
      if(q4<4) stageKV(q4+4); }
  }
    __builtin_amdgcn_s_setprio(PRIO_A);
  { auto ra=__builtin_amdgcn_permlane32_swap(__float_as_uint(oa0[10]),__float_as_uint(ob0[10]),false,false);
    auto rb=__builtin_amdgcn_permlane32_swap(__float_as_uint(ob0[10]),__float_as_uint(oa0[10]),false,false);
    const float x0=__uint_as_float(ra[0]), x1=__uint_as_float(rb[0]);
    const float ia=1.0f/(low? x0:x1), ib=1.0f/(low? x1:x0);
    WAIT_LGKM0(); BARRIER();
    LAS char* oa=(LAS char*)lds+(aw*2)*1152+r*36+hh*8; LAS char* ob=oa+1152;
    *(LAS unsigned*)(oa)=cvt2(oa0[0]*ia,oa0[1]*ia); *(LAS unsigned*)(oa+4)=cvt2(oa0[2]*ia,oa0[3]*ia); *(LAS unsigned*)(oa+16)=cvt2(oa0[4]*ia,oa0[5]*ia); *(LAS unsigned*)(oa+20)=cvt2(oa0[6]*ia,oa0[7]*ia);
    *(LAS unsigned*)(ob)=cvt2(ob0[0]*ib,ob0[1]*ib); *(LAS unsigned*)(ob+4)=cvt2(ob0[2]*ib,ob0[3]*ib); *(LAS unsigned*)(ob+16)=cvt2(ob0[4]*ib,ob0[5]*ib); *(LAS unsigned*)(ob+20)=cvt2(ob0[6]*ib,ob0[7]*ib);
    if(low){ *(LAS unsigned*)(oa+32)=cvt2(oa0[8]*ia,oa0[9]*ia); *(LAS unsigned*)(ob+32)=cvt2(ob0[8]*ib,ob0[9]*ib); }
    WAIT_LGKM0(); BARRIER();
    char* hf=ws+WS_HF+(long)tb*45*TST+(long)72*8192+(fb>>1)*1024+(fb&1)*128;
#pragma unroll
    for(int it=0;it<3;it++){ const int c=it*256+threadIdx.x; if(c<576){ const int i=c&7, ko=c>>3;
        st16(hf+(long)(ko>>2)*8192+(ko&3)*256+i*16,LD(L+i*1152+ko*16)); } }
  }
  WAIT_VM0(); BARRIER();
  if(threadIdx.x==0) set_flag(ws,tb*16+fb,FO,tag);
    if(id==0 && aw==0){
#pragma unroll 1
    for(int i=0;i<8;i++) poll_flags(ws,(i&3)*64+lane,i<4? FS:FW,tag);
    if(lane==0) __hip_atomic_store((gu32*)(ws+WS_EPOCH),tag,RLX_AGENT); }
}

extern "C" __global__ __launch_bounds__(256,2) void mega(In in,char* __restrict__ ws,u16* __restrict__ out){
  extern __shared__ __attribute__((aligned(16))) char lds[];
  const int wid=__builtin_amdgcn_readfirstlane(threadIdx.x>>6), lane=threadIdx.x&63;
  if(blockIdx.x<256){ __builtin_amdgcn_s_setprio(PRIO_G); g_role(in,ws,out,lds,blockIdx.x,wid,lane); }
  else {
    __builtin_amdgcn_s_setprio(PRIO_A); a_role(in,ws,lds,blockIdx.x-256,wid,lane); }
}

extern "C" void kernel_launch(void* const* d_in,const int* in_sizes,int n_in,void* d_out,int out_size,void* d_ws,size_t ws_size,hipStream_t stream){
  In in; in.x=(const u16*)d_in[0]; in.wq=(const u16*)d_in[1]; in.wk=(const u16*)d_in[2]; in.wv=(const u16*)d_in[3]; in.wo=(const u16*)d_in[4];
  in.w_in=(const u16*)d_in[6]; in.w_sp=(const u16*)d_in[7]; in.b_sp=(const u16*)d_in[8]; in.w_out=(const u16*)d_in[9];
  (void)hipFuncSetAttribute((const void*)mega,hipFuncAttributeMaxDynamicSharedMemorySize,LDS_BYTES);
  mega<<<512,256,LDS_BYTES,stream>>>(in,(char*)d_ws,(u16*)d_out);
}
```
